```python
import torch

def _softmax(x, axis=-1):
    return torch.softmax(x, dim=axis)
def _gelu(x):
    return torch.nn.functional.gelu(x, approximate="tanh")
def _silu(x):
    return torch.nn.functional.silu(x)
def _sigmoid(x):
    return torch.sigmoid(x)
def _layernorm(x, axis=-1, eps=1e-5):
    m = x.mean(dim=axis, keepdim=True)
    v = x.var(dim=axis, keepdim=True, unbiased=False)
    return (x - m) / torch.sqrt(v + eps)
def _scatter_add(tgt, idx, src, axis=0):
    return tgt.clone().index_add_(axis, idx, src)
def _segment_sum(src, seg, n):
    out = torch.zeros((n,) + src.shape[1:], dtype=src.dtype)
    return out.index_add_(0, seg, src)
def _pad(x, pw):
    flat = tuple(v for pair in reversed(pw) for v in pair)
    return torch.nn.functional.pad(x, flat)

kernel_name = 'standalone__deep_cross_xdeepfm__recsys'

def setup_inputs():

    _w = (lambda a: (lambda t: t.to(torch.bfloat16) if t.is_floating_point() else t)(torch.as_tensor(a)))
    B, F, D, L = 4096, 26, 16, 6
    V = min(1000, 100000)
    Din = F * D
    return {
        "idx": _w((lambda lo, hi, *s: torch.randint(lo, hi, s))(0, V, B, F).to(torch.int64)),
        "table": _w((lambda *s: torch.randn(*s, dtype=torch.bfloat16) * 0.8)(V, D)),
        "ws_cin": _w((lambda *s: torch.randn(*s, dtype=torch.bfloat16) * 0.4)(L, F, F, F) * (1.0 / F)),
        "w_cin_out": _w((lambda *s: torch.randn(*s, dtype=torch.bfloat16) * 0.4)(L * F) * ((L * F) ** -0.5)),
        "w_dnn1": _w((lambda *s: torch.randn(*s, dtype=torch.bfloat16) * 0.8)(Din, Din) * (Din ** -0.5)),
        "b_dnn1": _w((lambda *s: torch.randn(*s, dtype=torch.bfloat16) * 3.2)(Din) * 0.1),
        "w_dnn2": _w((lambda *s: torch.randn(*s, dtype=torch.bfloat16) * 0.4)(Din) * (Din ** -0.5)),
        "b_out": _w((lambda *s: torch.randn(*s, dtype=torch.bfloat16) * 0.4)(1)),
    }

def reference(idx, table, ws_cin, w_cin_out, w_dnn1, b_dnn1, w_dnn2, b_out):
    x0 = table[idx]
    B = x0.shape[0]
    L = ws_cin.shape[0]
    xk = x0
    pooled = []
    for l in range(L):
        xk = torch.einsum('bid,bjd,hij->bhd', xk, x0, ws_cin[l])
        pooled.append(xk.sum(axis=-1))
    p_plus = torch.stack(pooled, axis=1).reshape(B, -1)
    logit_cin = p_plus @ w_cin_out
    h = x0.reshape(B, -1)
    h = _gelu(h @ w_dnn1 + b_dnn1)
    logit_dnn = h @ w_dnn2
    return logit_cin + logit_dnn + b_out
```

```cpp
#include <hip/hip_runtime.h>
#include <hip/hip_bf16.h>

static constexpr int B_ = 4096, F_ = 26, D_ = 16, L_ = 6, DIN = 416;

__device__ __forceinline__ float bf2f(unsigned short u) { return __uint_as_float(((unsigned)u) << 16); }

__global__ __launch_bounds__(256) void naive_kernel(const int* __restrict__ idx, const unsigned short* __restrict__ table,
    const unsigned short* __restrict__ W, const unsigned short* __restrict__ wout, const unsigned short* __restrict__ w1,
    const unsigned short* __restrict__ b1, const unsigned short* __restrict__ w2, const unsigned short* __restrict__ bout,
    __hip_bfloat16* __restrict__ out) {
  __shared__ float x0[F_ * D_], xk[F_ * D_], xn[F_ * D_], red[256];
  int b = blockIdx.x, t = threadIdx.x;
  for (int e = t; e < F_ * D_; e += 256) {
    int f = e / D_, d = e % D_;
    float v = bf2f(table[idx[b * F_ + f] * D_ + d]);
    x0[e] = v; xk[e] = v;
  }
  __syncthreads();
  float acc = 0.f;
  for (int l = 0; l < L_; ++l) {
    for (int e = t; e < F_ * D_; e += 256) {
      int h = e / D_, d = e % D_;
      float s = 0.f;
      for (int i = 0; i < F_; ++i) {
        float m = 0.f;
        for (int j = 0; j < F_; ++j) m += bf2f(W[((l * F_ + h) * F_ + i) * F_ + j]) * x0[j * D_ + d];
        s += m * xk[i * D_ + d];
      }
      xn[e] = s;
      acc += s * bf2f(wout[l * F_ + h]);
    }
    __syncthreads();
    for (int e = t; e < F_ * D_; e += 256) xk[e] = xn[e];
    __syncthreads();
  }
  for (int n = t; n < DIN; n += 256) {
    float s = 0.f;
    for (int k = 0; k < DIN; ++k) s += x0[k] * bf2f(w1[k * DIN + n]);
    s += bf2f(b1[n]);
    float u = 0.7978845608028654f * (s + 0.044715f * s * s * s);
    float g = 0.5f * s * (1.f + tanhf(u));
    acc += g * bf2f(w2[n]);
  }
  red[t] = acc;
  __syncthreads();
  for (int s = 128; s > 0; s >>= 1) { if (t < s) red[t] += red[t + s]; __syncthreads(); }
  if (t == 0) out[b] = __float2bfloat16(red[0] + bf2f(bout[0]));
}

extern "C" void kernel_launch(void* const* d_in, const int* in_sizes, int n_in,
                              void* d_out, int out_size, void* d_ws, size_t ws_size,
                              hipStream_t stream) {
  naive_kernel<<<B_, 256, 0, stream>>>((const int*)d_in[0], (const unsigned short*)d_in[1], (const unsigned short*)d_in[2],
    (const unsigned short*)d_in[3], (const unsigned short*)d_in[4], (const unsigned short*)d_in[5], (const unsigned short*)d_in[6],
    (const unsigned short*)d_in[7], (__hip_bfloat16*)d_out);
}
```

```cpp
#include <hip/hip_runtime.h>
#include <hip/hip_bf16.h>

typedef _Float16 h2 __attribute__((ext_vector_type(2)));
typedef _Float16 h8 __attribute__((ext_vector_type(8)));
typedef __bf16 b8 __attribute__((ext_vector_type(8)));
typedef short s4 __attribute__((ext_vector_type(4)));
typedef float f4 __attribute__((ext_vector_type(4)));
typedef float f16v __attribute__((ext_vector_type(16)));
typedef unsigned u32;
typedef unsigned u2 __attribute__((ext_vector_type(2)));
typedef unsigned u4 __attribute__((ext_vector_type(4)));
typedef unsigned u4u __attribute__((ext_vector_type(4), aligned(4)));
#define DI __device__ __forceinline__
#define LDSP(T, off) ((__attribute__((address_space(3))) T*)(uintptr_t)(off))

static constexpr int F_ = 26, DIN = 416;
static constexpr int FS = 848;
static constexpr int FRB = 43 * FS;
static constexpr int R0 = 0, R1 = FRB, R2 = 2 * FRB, R3 = 3 * FRB;
static constexpr int MISC = 4 * FRB;
static constexpr int U5F = MISC;
static constexpr int WOF = U5F + 3328;
static constexpr int RED = WOF + 640;
static constexpr int LDS_BYTES = RED + 576;

__host__ __device__ constexpr int rof(int il) { return (il % 3 == 0) ? (il == 12 ? 12 : il / 3) : 4 + 2 * (il / 3) + (il % 3 - 1); }

DI float bflo(u32 w) { return __uint_as_float(w << 16); }
DI float bfhi(u32 w) { return __uint_as_float(w & 0xffff0000u); }
DI u32 cvt_w(u32 w) { return __builtin_bit_cast(u32, __builtin_amdgcn_cvt_pkrtz(bflo(w), bfhi(w))); }

DI h8 zprod(int m, const h2 (&x0p)[13], const h2 (&xkp)[7]) {
  h2 z[4];
#pragma unroll
  for (int q = 0; q < 4; ++q) {
    const int t = 4 * m + q;
    if (t < 169) {
      const int il = t / 13, jp = t % 13;
      h2 xk = xkp[il >> 1];
      h2 bc = (il & 1) ? __builtin_shufflevector(xk, xk, 1, 1) : __builtin_shufflevector(xk, xk, 0, 0);
      z[q] = bc * x0p[jp];
    } else {
      z[q] = (h2){(_Float16)0, (_Float16)0};
    }
  }
  return (h8){z[0][0], z[0][1], z[1][0], z[1][1], z[2][0], z[2][1], z[3][0], z[3][1]};
}
DI void cin_layer(u32 fragaddr, const h2 (&x0p)[13], const h2 (&xkp)[7], f16v& acc) {
  asm volatile("" : "+v"(fragaddr));
  h8 a[43];
  a[0] = *LDSP(const h8, fragaddr);
  a[1] = *LDSP(const h8, fragaddr + FS);
  a[2] = *LDSP(const h8, fragaddr + 2 * FS);
#pragma unroll
  for (int i = 0; i < 16; ++i) acc[i] = 0.f;
  h8 z = zprod(0, x0p, xkp);
  __builtin_amdgcn_sched_barrier(0);
#pragma unroll
  for (int m = 0; m < 43; ++m) {
    acc = __builtin_amdgcn_mfma_f32_32x32x16_f16(a[m], z, acc, 0, 0, 0);
    asm volatile("" : "+v"(acc));
    if (m + 1 < 43) z = zprod(m + 1, x0p, xkp);
    if (m + 3 < 43) a[m + 3] = *LDSP(const h8, fragaddr + (m + 3) * FS);
    __builtin_amdgcn_sched_barrier(0);
  }
}

DI float swap32_max(float v) {
  u32 x = __float_as_uint(v);
  auto r = __builtin_amdgcn_permlane32_swap(x, x, false, false);
  return fmaxf(__uint_as_float(r[0]), __uint_as_float(r[1]));
}
DI float swap32_add(float v) {
  u32 x = __float_as_uint(v);
  auto r = __builtin_amdgcn_permlane32_swap(x, x, false, false);
  return __uint_as_float(r[0]) + __uint_as_float(r[1]);
}
DI float row16_sum(float v) {
  v += __builtin_bit_cast(float, __builtin_amdgcn_update_dpp(0, __builtin_bit_cast(int, v), 0x128, 0xf, 0xf, false));
  v += __builtin_bit_cast(float, __builtin_amdgcn_update_dpp(0, __builtin_bit_cast(int, v), 0x124, 0xf, 0xf, false));
  v += __builtin_bit_cast(float, __builtin_amdgcn_update_dpp(0, __builtin_bit_cast(int, v), 0x122, 0xf, 0xf, false));
  v += __builtin_bit_cast(float, __builtin_amdgcn_update_dpp(0, __builtin_bit_cast(int, v), 0x121, 0xf, 0xf, false));
  return v;
}

DI void layer_post(const f16v& acc, u32 wofaddr, float& p, float& inv, float inv_s0, h2 (&xkp)[7]) {
  f4 w0 = *LDSP(const f4, wofaddr), w1 = *LDSP(const f4, wofaddr + 16), w2 = *LDSP(const f4, wofaddr + 32), w3 = *LDSP(const f4, wofaddr + 48);
  float w[16] = {w0[0], w0[1], w0[2], w0[3], w1[0], w1[1], w1[2], w1[3], w2[0], w2[1], w2[2], w2[3], w3[0], w3[1], w3[2], w3[3]};
  float d = 0.f, m = 0x1p-118f;
#pragma unroll
  for (int r = 0; r < 13; ++r) { d = fmaf(w[r], acc[r], d); m = fmaxf(m, fabsf(acc[r])); }
  p = fmaf(d, inv, p);
  asm volatile("" : "+v"(p));
  m = swap32_max(m);
  u32 mb = __float_as_uint(m) & 0x7f800000u;
  float s = __uint_as_float(0x82800000u - mb);
  inv = inv * __uint_as_float(mb - (7u << 23)) * inv_s0;
#pragma unroll
  for (int c = 0; c < 7; ++c) {
    xkp[c][0] = (_Float16)(acc[rof(2 * c)] * s);
    xkp[c][1] = (2 * c + 1 < 13) ? (_Float16)(acc[rof(2 * c + 1 < 13 ? 2 * c + 1 : 0)] * s) : (_Float16)0;
  }
}

DI void load_raw(const char* Wl, u32 goff, bool act4, u4 (&raw)[5]) {
#pragma unroll
  for (int k = 0; k < 4; ++k) raw[k] = *(const u4u*)(Wl + goff + k * 11 * 676);
  raw[4] = *(const u4u*)(Wl + (act4 ? goff + 44 * 676 : goff));
}
DI void conv_store(const u4 (&raw)[5], u32 lde, u32 ldo, bool act, bool act4) {
  asm volatile("" : "+v"(lde), "+v"(ldo));
#pragma unroll
  for (int k = 0; k < 5; ++k) {
    u4 o;
#pragma unroll
    for (int q = 0; q < 4; ++q) o[q] = cvt_w(raw[k][q]);
    u32 a = (k & 1) ? ldo + (11 * (k - 1) / 2) * 16 : lde + (11 * k / 2) * 16;
    if (k < 4 ? act : act4) *LDSP(u4, a) = o;
  }
}

__global__ __launch_bounds__(512, 2) void xdfm_kernel(const int* __restrict__ idx, const char* __restrict__ table,
    const char* __restrict__ W, const unsigned short* __restrict__ wout, const char* __restrict__ w1,
    const unsigned short* __restrict__ b1, const unsigned short* __restrict__ w2, const unsigned short* __restrict__ bout,
    __hip_bfloat16* __restrict__ out) {
  __shared__ __attribute__((aligned(16))) char smem[LDS_BYTES];
  const u32 sbase = (u32)(uintptr_t)(__attribute__((address_space(3))) char*)smem;
  const int tid = threadIdx.x, lane = tid & 63, w = __builtin_amdgcn_readfirstlane(tid >> 6);
  const int b0 = blockIdx.x * 16;
  const int hh = lane >> 5;

#pragma unroll
  for (int i = 0; i < 4; ++i) {
    int off = (w * 4 + i) * 1024 + lane * 16;
    int goff = off < 32000 ? off : 0;
    __builtin_amdgcn_global_load_lds((const __attribute__((address_space(1))) void*)(table + goff),
                                     (__attribute__((address_space(3))) void*)(smem + R0 + (w * 4 + i) * 1024), 16, 0, 0);
  }
  int ci[7];
  {
    const int* ip = idx + (b0 + 2 * w + ((lane >> 4) & 1)) * F_;
    const int q = (lane & 15) >> 2;
#pragma unroll
    for (int t = 0; t < 6; ++t) ci[t] = ip[4 * t + q];
    ci[6] = ip[24 + (q & 1)];
  }
  int di[13];
  {
    const int* ip = idx + (b0 + (lane & 15)) * F_ + (lane >> 5);
#pragma unroll
    for (int s = 0; s < 13; ++s) di[s] = ip[2 * s];
  }
  const int sb = (tid * 1525) >> 16;
  const int mm = tid - 43 * sb;
  const bool act = tid < 473, act4 = tid < 344;
  const u32 goff = act ? (u32)(676 * sb + 16 * mm) : 0u;
  const u32 lde = sbase + mm * FS + (26 * (sb & 1) + (sb >> 1)) * 16;
  const u32 ldo = sbase + mm * FS + (26 * ((sb + 11) & 1) + ((sb + 11) >> 1)) * 16;
  u4 rawA[5], rawB[5], rawC[5];
  load_raw(W + 0 * 35152, goff, act4, rawA);
  load_raw(W + 1 * 35152, goff, act4, rawB);
  load_raw(W + 2 * 35152, goff, act4, rawC);
  conv_store(rawA, lde + R1, ldo + R1, act, act4);
  load_raw(W + 3 * 35152, goff, act4, rawA);
  conv_store(rawB, lde + R2, ldo + R2, act, act4);
  load_raw(W + 4 * 35152, goff, act4, rawB);
  conv_store(rawC, lde + R3, ldo + R3, act, act4);
  if (tid < 160) {
    int l = tid >> 5, h2_ = (tid >> 4) & 1, r = tid & 15;
    int il = r < 4 ? 3 * r : (r < 12 ? 3 * ((r - 4) >> 1) + 1 + ((r - 4) & 1) : 12);
    float v = r <= 12 ? __uint_as_float((u32)wout[l * F_ + 13 * h2_ + il] << 16) : 0.f;
    *LDSP(float, sbase + WOF + tid * 4) = v;
  }
  if (tid < 416) {
    int i = tid >> 4, jp = tid & 15;
    float s0 = 0.f, s1 = 0.f;
    if (jp < 13) {
      const u32* wp = (const u32*)(W + 5 * 35152) + i * 13 + jp;
#pragma unroll
      for (int h = 0; h < F_; ++h) {
        u32 v = wp[h * 338];
        float wh = __uint_as_float((u32)wout[5 * F_ + h] << 16);
        s0 = fmaf(bflo(v), wh, s0); s1 = fmaf(bfhi(v), wh, s1);
      }
    }
    u32 h0 = __float_as_uint(s0) & 0xffff0000u, h1 = __float_as_uint(s1) & 0xffff0000u;
    float l0 = s0 - __uint_as_float(h0), l1 = s1 - __uint_as_float(h1);
    u32 hi = (h0 >> 16) | h1;
    u32 lo = (__float_as_uint(l0) >> 16) | (__float_as_uint(l1) & 0xffff0000u);
    u32 a = sbase + U5F + (jp >> 2) * 832 + i * 16 + (jp & 3) * 4;
    *LDSP(u32, a) = hi;
    *LDSP(u32, a + 26 * 16) = lo;
  }
  __syncthreads();

  u32 xb[16];
  {
    const u32 pa = sbase + R0 + (lane & 3) * 8;
#pragma unroll
    for (int t = 0; t < 7; ++t) {
      s4 v = __builtin_amdgcn_ds_read_tr16_b64_v4i16(LDSP(s4, pa + ci[t] * 32));
      u2 u = __builtin_bit_cast(u2, v);
      xb[2 * t] = u[0]; xb[2 * t + 1] = u[1];
    }
    xb[14] = 0; xb[15] = 0;
  }
  u4 xd[13];
  {
    const u32 pa = sbase + R0 + ((lane >> 4) & 1) * 16;
#pragma unroll
    for (int s = 0; s < 13; ++s) xd[s] = *LDSP(const u4, pa + di[s] * 32);
  }
  __syncthreads();
  conv_store(rawA, lde + R0, ldo + R0, act, act4);

  h2 x0p[13], xkp[7];
  float inv_s0, inv, p = 0.f;
  {
    float xf[26];
    float m = 0x1p-118f;
#pragma unroll
    for (int j = 0; j < 13; ++j) { xf[2 * j] = bflo(xb[j]); xf[2 * j + 1] = bfhi(xb[j]); m = fmaxf(m, fmaxf(fabsf(xf[2 * j]), fabsf(xf[2 * j + 1]))); }
    u32 mb = __float_as_uint(m) & 0x7f800000u;
    float s = __uint_as_float(0x82000000u - mb);
    inv_s0 = __uint_as_float(mb - (6u << 23));
    inv = inv_s0 * inv_s0;
#pragma unroll
    for (int j = 0; j < 13; ++j) { x0p[j][0] = (_Float16)(xf[2 * j] * s); x0p[j][1] = (_Float16)(xf[2 * j + 1] * s); }
#pragma unroll
    for (int c = 0; c < 7; ++c) {
      u32 lo = __builtin_bit_cast(u32, x0p[c]);
      u32 a = __builtin_bit_cast(u32, x0p[6 + c]), b = __builtin_bit_cast(u32, x0p[c < 6 ? 7 + c : 12]);
      u32 hi = (a >> 16) | (b << 16);
      xkp[c] = __builtin_bit_cast(h2, hh ? hi : lo);
    }
  }
  u32 fr;
  {
    int rho = lane & 31, r = (rho & 3) + 4 * (rho >> 3), hp = (rho >> 2) & 1;
    int il = r < 4 ? 3 * r : (r < 12 ? 3 * ((r - 4) >> 1) + 1 + ((r - 4) & 1) : 12);
    fr = sbase + (26 * hh + 13 * hp + il) * 16;
  }
  const u32 wof = sbase + WOF + hh * 64;
  f16v acc;
  cin_layer(fr + R1, x0p, xkp, acc);
  layer_post(acc, wof, p, inv, inv_s0, xkp);
  __syncthreads();
  conv_store(rawB, lde + R1, ldo + R1, act, act4);
  cin_layer(fr + R2, x0p, xkp, acc);
  layer_post(acc, wof + 128, p, inv, inv_s0, xkp);
  cin_layer(fr + R3, x0p, xkp, acc);
  layer_post(acc, wof + 256, p, inv, inv_s0, xkp);
  __syncthreads();
  cin_layer(fr + R0, x0p, xkp, acc);
  layer_post(acc, wof + 384, p, inv, inv_s0, xkp);
  cin_layer(fr + R1, x0p, xkp, acc);
  {
    f16v u;
    f4 w0 = *LDSP(const f4, wof + 512), w1_ = *LDSP(const f4, wof + 528), w2_ = *LDSP(const f4, wof + 544), w3 = *LDSP(const f4, wof + 560);
#pragma unroll
    for (int i = 0; i < 4; ++i) { u[i] = w0[i]; u[4 + i] = w1_[i]; u[8 + i] = w2_[i]; u[12 + i] = w3[i]; }
#pragma unroll
    for (int m4 = 0; m4 < 4; ++m4) {
      b8 a = *LDSP(const b8, fr + U5F + m4 * 832);
      u4 bb = {xb[4 * m4], xb[4 * m4 + 1], xb[4 * m4 + 2], xb[4 * m4 + 3]};
      u = __builtin_amdgcn_mfma_f32_32x32x16_bf16(a, __builtin_bit_cast(b8, bb), u, 0, 0, 0);
    }
    float d = 0.f;
#pragma unroll
    for (int r = 0; r < 13; ++r) d = fmaf(u[r], acc[r], d);
    p = fmaf(d, inv, p);
  }
  p = swap32_add(row16_sum(p));
  if ((lane & 47) == 0) *LDSP(float, sbase + RED + 8 * 64 + (2 * w + (lane >> 4)) * 4) = p;

  {
    const int c = lane & 15, g = lane >> 4, cc = c < 13 ? c : 12;
    const int n0 = 52 * w + 4 * cc;
    const char* wp = w1 + (8 * g) * 832 + n0 * 2;
    u2 bv = *(const u2*)(b1 + n0), wv = *(const u2*)(w2 + n0);
    float bias[4] = {bflo(bv[0]), bfhi(bv[0]), bflo(bv[1]), bfhi(bv[1])};
    float wo[4] = {bflo(wv[0]), bfhi(wv[0]), bflo(wv[1]), bfhi(wv[1])};
    f4 da[4];
#pragma unroll
    for (int t = 0; t < 4; ++t) da[t] = (f4){bias[t], bias[t], bias[t], bias[t]};
#pragma unroll
    for (int s = 0; s < 13; ++s) {
      u2 r[8];
#pragma unroll
      for (int j = 0; j < 8; ++j) r[j] = *(const u2*)(wp + (32 * s + j) * 832);
#pragma unroll
      for (int t = 0; t < 4; ++t) {
        u4 f;
#pragma unroll
        for (int q = 0; q < 4; ++q) f[q] = __builtin_amdgcn_perm(r[2 * q + 1][t >> 1], r[2 * q][t >> 1], (t & 1) ? 0x07060302u : 0x05040100u);
        da[t] = __builtin_amdgcn_mfma_f32_16x16x32_bf16(__builtin_bit_cast(b8, xd[s]), __builtin_bit_cast(b8, f), da[t], 0, 0, 0);
      }
    }
    float o[4] = {0.f, 0.f, 0.f, 0.f};
#pragma unroll
    for (int t = 0; t < 4; ++t) {
      float wt = c < 13 ? wo[t] : 0.f;
#pragma unroll
      for (int r = 0; r < 4; ++r) {
        float x = da[t][r];
        float q = x * fmaf(x * x, -0.1029432397f, -2.302208198f);
        float e = __builtin_amdgcn_exp2f(q);
        float gl = x * __builtin_amdgcn_rcpf(1.f + e);
        o[r] = fmaf(gl, wt, o[r]);
      }
    }
#pragma unroll
    for (int r = 0; r < 4; ++r) o[r] = row16_sum(o[r]);
    if (c == 0) *LDSP(f4, sbase + RED + w * 64 + g * 16) = (f4){o[0], o[1], o[2], o[3]};
  }
  __syncthreads();
  if (tid < 16) {
    float s = __uint_as_float((u32)bout[0] << 16);
#pragma unroll
    for (int k = 0; k < 9; ++k) s += *LDSP(const float, sbase + RED + k * 64 + tid * 4);
    out[b0 + tid] = __float2bfloat16(s);
  }
}

extern "C" void kernel_launch(void* const* d_in, const int* in_sizes, int n_in,
                              void* d_out, int out_size, void* d_ws, size_t ws_size,
                              hipStream_t stream) {
  xdfm_kernel<<<256, 512, 0, stream>>>((const int*)d_in[0], (const char*)d_in[1], (const char*)d_in[2],
    (const unsigned short*)d_in[3], (const char*)d_in[4], (const unsigned short*)d_in[5], (const unsigned short*)d_in[6],
    (const unsigned short*)d_in[7], (__hip_bfloat16*)d_out);
}
```

```cpp
#include <hip/hip_runtime.h>
#include <hip/hip_bf16.h>

typedef unsigned u32;
static constexpr int kLds = 154816;

__device__ __forceinline__ float bf2f(unsigned short u) { return __uint_as_float(((unsigned)u) << 16); }

__global__ __launch_bounds__(512, 2) __attribute__((aligned(4096))) void xdfm_kernel(const int* idx, const unsigned short* table,
    const unsigned short* W, const unsigned short* wout, const unsigned short* w1,
    const unsigned short* b1, const unsigned short* w2, const unsigned short* bout,
    __hip_bfloat16* out, char* ws) {
  __shared__ __attribute__((aligned(16))) char smem[kLds];
  unsigned bad;
  const unsigned tid = threadIdx.x, blk = blockIdx.x;
  const void* kp = (const void*)__builtin_amdgcn_kernarg_segment_ptr();
  asm volatile(
    "s_load_dwordx16 s[8:23], s[0:1], 0x0\n"
    "s_load_dwordx4 s[24:27], s[0:1], 0x40\n"
    "s_getpc_b64 s[44:45]\n"
    "v_lshrrev_b32 v4, 6, v0\n"
    "v_and_b32 v1, 63, v0\n"
    "v_readfirstlane_b32 s30, v4\n"
    "s_bfe_u32 s43, s2, 0x20003\n"
    "v_lshrrev_b32 v11, 3, v0\n"
    "v_lshl_add_u32 v11, s43, 6, v11\n"
    "v_lshlrev_b32 v11, 7, v11\n"
    "global_load_dword v16, v11, s[44:45] offset:1024\n"
    "v_lshlrev_b32 v2, 4, v1\n"
    "s_sub_u32 s58, s2, 26\n"
    "s_cselect_b32 s36, 8, 0\n"
    "s_and_b32 s31, s30, 3\n"
    "s_cmp_lt_u32 s30, 4\n"
    "s_cbranch_scc1 LC3_%=\n"
    "s_add_u32 s36, s36, s30\n"
    "s_cmp_lt_u32 s36, 8\n"
    "s_cbranch_scc1 LH1_%=\n"
    "s_cmp_lt_u32 s36, 14\n"
    "s_cbranch_scc1 LPQ7_%=\n"
    "s_cmp_eq_u32 s36, 14\n"
    "s_cbranch_scc1 LP26_%=\n"
    "LP010_%=:\n"
    "s_setprio 3\n"
    "s_mul_i32 s40, s2, 1352\n"
    "v_min_u32 v4, 45, v1\n"
    "v_mul_u32_u24 v5, 158, v4\n"
    "v_lshrrev_b32 v5, 10, v5\n"
    "v_lshlrev_b32 v7, 1, v4\n"
    "v_mad_i32_i24 v7, v5, -13, v7\n"
    "v_add_u32 v14, 1, v7\n"
    "v_subrev_u32 v8, 12, v7\n"
    "v_min_u32 v8, v14, v8\n"
    "v_sub_u32 v6, v14, v8\n"
    "v_and_b32 v6, 1, v6\n"
    "v_add_u32 v6, v6, v5\n"
    "v_add_u32 v9, v7, v5\n"
    "v_subrev_u32 v14, 13, v9\n"
    "v_min_u32 v9, v9, v14\n"
    "v_mad_u32_u24 v10, v7, 26, v9\n"
    "v_mad_u32_u24 v12, v9, 26, v7\n"
    "v_lshlrev_b32 v10, 2, v10\n"
    "v_lshlrev_b32 v12, 2, v12\n"
    "v_add_u32 v9, v8, v6\n"
    "v_subrev_u32 v14, 13, v9\n"
    "v_min_u32 v9, v9, v14\n"
    "v_mad_u32_u24 v11, v8, 26, v9\n"
    "v_mad_u32_u24 v13, v9, 26, v8\n"
    "v_lshlrev_b32 v11, 2, v11\n"
    "v_lshlrev_b32 v13, 2, v13\n"
    "s_waitcnt lgkmcnt(0)\n"
    "s_load_dword s56, s[26:27], 0x0\n"
    "s_add_u32 s42, s12, s40\n"
    "s_addc_u32 s43, s13, 0\n"
    "global_load_dword v24, v10, s[42:43] offset:0\n"
    "global_load_dword v25, v10, s[42:43] offset:52\n"
    "global_load_dword v28, v12, s[42:43] offset:0\n"
    "global_load_dword v29, v12, s[42:43] offset:52\n"
    "global_load_dword v26, v11, s[42:43] offset:0\n"
    "global_load_dword v27, v11, s[42:43] offset:52\n"
    "global_load_dword v30, v13, s[42:43] offset:0\n"
    "global_load_dword v31, v13, s[42:43] offset:52\n"
    "s_barrier\n"
    "s_branch LS42_%=\n"
    "LH1_%=:\n"
    "s_cmp_ge_u32 s58, 224\n"
    "s_cbranch_scc1 LL4_%=\n"
    "s_and_b32 s29, s58, 31\n"
    "s_lshl_b32 s29, s29, 2\n"
    "s_add_u32 s29, s29, s31\n"
    "s_mul_i32 s54, s29, 3328\n"
    "s_lshl_b32 s37, s31, 12\n"
    "s_add_u32 m0, s37, 19456\n"
    "v_min_u32 v14, 0xf0, v2\n"
    "s_lshr_b32 s57, s58, 5\n"
    "v_lshl_add_u32 v7, s57, 6, v1\n"
    "v_min_u32 v8, 415, v7\n"
    "v_cmp_eq_u32 s[50:51], v8, v7\n"
    "v_lshlrev_b32 v8, 1, v8\n"
    "s_waitcnt lgkmcnt(0)\n"
    "s_load_dword s56, s[26:27], 0x0\n"
    "s_add_u32 s38, s8, s54\n"
    "s_addc_u32 s39, s9, 0\n"
    "global_load_lds_dwordx4 v2, s[38:39] offset:0\n"
    "global_load_lds_dwordx4 v2, s[38:39] offset:1024\n"
    "global_load_lds_dwordx4 v2, s[38:39] offset:2048\n"
    "global_load_lds_dwordx4 v14, s[38:39] offset:3072\n"
    "s_bitcmp1_b32 s30, 0\n"
    "s_cselect_b64 s[42:43], s[20:21], s[18:19]\n"
    "s_cselect_b32 s44, 256, 0\n"
    "global_load_ushort v23, v8, s[42:43]\n"
    "s_cmp_lt_u32 s57, 6\n"
    "s_cselect_b32 s46, 7, 3\n"
    "v_lshrrev_b32 v219, 2, v1\n"
    "v_and_b32 v219, 4, v219\n"
    "v_xor_b32 v219, v219, v1\n"
    "v_and_b32 v219, s46, v219\n"
    "v_lshrrev_b32 v218, 3, v1\n"
    "v_mul_u32_u24 v218, 832, v218\n"
    "v_lshl_add_u32 v218, v219, 4, v218\n"
    "v_add_u32 v219, 11264, v218\n"
    "s_lshr_b32 s36, s31, 1\n"
    "s_mul_i32 s36, s36, 24\n"
    "s_add_u32 s36, s36, s31\n"
    "s_lshl_b32 s46, s57, 7\n"
    "s_mul_i32 s47, s36, 6656\n"
    "s_add_u32 s46, s46, s47\n"
    "s_add_u32 s46, s16, s46\n"
    "s_addc_u32 s47, s17, 0\n"
    "s_lshl_b32 s36, s36, 10\n"
    "s_add_u32 m0, s36, 89088\n"
    "s_barrier\n"
    "global_load_lds_dwordx4 v218, s[46:47] offset:0\n"
    "global_load_lds_dwordx4 v219, s[46:47] offset:2048\n"
    "s_add_u32 m0, s36, 93184\n"
    "s_add_u32 s46, s46, 26624\n"
    "s_addc_u32 s47, s47, 0\n"
    "global_load_lds_dwordx4 v218, s[46:47] offset:0\n"
    "global_load_lds_dwordx4 v219, s[46:47] offset:2048\n"
    "s_add_u32 m0, s36, 97280\n"
    "s_add_u32 s46, s46, 26624\n"
    "s_addc_u32 s47, s47, 0\n"
    "global_load_lds_dwordx4 v218, s[46:47] offset:0\n"
    "global_load_lds_dwordx4 v219, s[46:47] offset:2048\n"
    "s_add_u32 m0, s36, 101376\n"
    "s_add_u32 s46, s46, 26624\n"
    "s_addc_u32 s47, s47, 0\n"
    "global_load_lds_dwordx4 v218, s[46:47] offset:0\n"
    "s_branch LS34_%=\n"
    "LC3_%=:\n"
    "v_and_b32 v7, 60, v0\n"
    "v_add_u32 v7, 153728, v7\n"
    "v_mov_b32 v85, 0\n"
    "ds_write_b32 v7, v85\n"
    "s_mul_i32 s40, s2, 1664\n"
    "v_lshrrev_b32 v10, 4, v0\n"
    "v_bfe_u32 v6, v0, 2, 2\n"
    "v_mul_u32_u24 v6, 28, v6\n"
    "v_min_u32 v6, 0x4c, v6\n"
    "v_mul_u32_u24 v4, 0x68, v10\n"
    "v_add3_u32 v5, v4, v6, s40\n"
    "v_bfe_u32 v3, v0, 4, 1\n"
    "v_xor_b32 v3, v3, v1\n"
    "v_lshlrev_b32 v3, 4, v3\n"
    "s_lshl_b32 s36, s31, 13\n"
    "s_cmp_eq_u32 s31, 3\n"
    "s_movk_i32 s37, 1008\n"
    "s_cselect_b32 s37, 240, s37\n"
    "v_min_u32 v15, s37, v3\n"
    "s_waitcnt lgkmcnt(0)\n"
    "s_load_dword s56, s[26:27], 0x0\n"
    "global_load_dwordx4 v[236:239], v5, s[8:9]\n"
    "global_load_dwordx3 v[240:242], v5, s[8:9] offset:16\n"
    "s_barrier\n"
    "s_add_u32 s38, s10, s36\n"
    "s_addc_u32 s39, s11, 0\n"
    "s_add_u32 m0, s36, 56320\n"
    "s_nop 0\n"
    "global_load_lds_dwordx4 v3, s[38:39] offset:0\n"
    "global_load_lds_dwordx4 v3, s[38:39] offset:1024\n"
    "global_load_lds_dwordx4 v3, s[38:39] offset:2048\n"
    "global_load_lds_dwordx4 v3, s[38:39] offset:3072\n"
    "s_add_u32 s38, s38, 4096\n"
    "s_addc_u32 s39, s39, 0\n"
    "s_add_u32 m0, s36, 60416\n"
    "s_nop 0\n"
    "global_load_lds_dwordx4 v3, s[38:39] offset:0\n"
    "global_load_lds_dwordx4 v3, s[38:39] offset:1024\n"
    "global_load_lds_dwordx4 v3, s[38:39] offset:2048\n"
    "global_load_lds_dwordx4 v15, s[38:39] offset:3072\n"
    "s_mul_i32 s41, s2, 1536\n"
    "v_min_u32 v4, 11, v10\n"
    "v_lshl_add_u32 v4, v4, 7, s41\n"
    "v_min_u32 v4, 345984, v4\n"
    "global_load_dword v104, v4, s[16:17]\n"
    "s_mov_b32 s46, 0x54219630\n"
    "s_mov_b32 s47, 0xccccba87\n"
    "v_lshlrev_b32 v8, 2, v1\n"
    "v_lshrrev_b64 v[128:129], v8, s[46:47]\n"
    "v_and_b32 v8, 15, v128\n"
    "v_lshrrev_b32 v9, 4, v1\n"
    "v_mad_u32_u24 v9, v9, 13, v8\n"
    "v_lshlrev_b32 v9, 1, v9\n"
    "global_load_ushort v124, v9, s[14:15] offset:0\n"
    "global_load_ushort v125, v9, s[14:15] offset:104\n"
    "s_branch LS11_%=\n"
    "LP26_%=:\n"
    "s_setprio 3\n"
    "s_mul_i32 s40, s2, 1352\n"
    "s_add_u32 s40, s40, 35152\n"
    "v_subrev_u32 v64, 43, v1\n"
    "v_min_u32 v64, v1, v64\n"
    "v_sub_u32 v66, v1, v64\n"
    "v_and_b32 v66, 1, v66\n"
    "v_add_u32 v65, 21, v1\n"
    "v_min_u32 v65, 42, v65\n"
    "v_mov_b32 v67, 1\n"
    "v_mul_u32_u24 v68, 676, v66\n"
    "v_lshl_add_u32 v68, v64, 4, v68\n"
    "v_lshlrev_b32 v69, 4, v65\n"
    "s_waitcnt lgkmcnt(0)\n"
    "s_load_dword s56, s[26:27], 0x0\n"
    "s_add_u32 s46, s12, s40\n"
    "s_addc_u32 s47, s13, 0\n"
    "global_load_dwordx4 v[46:49], v68, s[46:47] offset:0\n"
    "global_load_dwordx4 v[50:53], v69, s[46:47] offset:676\n"
    "s_barrier\n"
    "s_branch LS43_%=\n"
    "LPQ7_%=:\n"
    "s_setprio 3\n"
    "v_and_b32 v136, 15, v1\n"
    "v_lshrrev_b32 v137, 4, v1\n"
    "v_min_u32 v138, 1, v137\n"
    "v_lshlrev_b32 v4, 1, v137\n"
    "v_lshlrev_b32 v5, 1, v138\n"
    "v_mul_u32_u24 v6, 1352, v137\n"
    "v_mul_u32_u24 v7, 1352, v138\n"
    "v_lshl_add_u32 v143, s31, 4, v136\n"
    "v_min_u32 v143, 25, v143\n"
    "v_lshl_add_u32 v139, v143, 1, v6\n"
    "v_lshl_add_u32 v140, v143, 1, v7\n"
    "s_mul_i32 s40, s2, 52\n"
    "s_add_u32 s40, s40, 175760\n"
    "s_waitcnt lgkmcnt(0)\n"
    "s_load_dword s56, s[26:27], 0x0\n"
    "s_barrier\n"
    "s_add_u32 s42, s12, s40\n"
    "s_addc_u32 s43, s13, 0\n"
    "global_load_ushort v45, v139, s[42:43]\n"
    "s_add_u32 s42, s42, 5408\n"
    "s_addc_u32 s43, s43, 0\n"
    "global_load_ushort v46, v139, s[42:43]\n"
    "s_add_u32 s42, s42, 5408\n"
    "s_addc_u32 s43, s43, 0\n"
    "global_load_ushort v47, v139, s[42:43]\n"
    "s_add_u32 s42, s42, 5408\n"
    "s_addc_u32 s43, s43, 0\n"
    "global_load_ushort v48, v139, s[42:43]\n"
    "s_add_u32 s42, s42, 5408\n"
    "s_addc_u32 s43, s43, 0\n"
    "global_load_ushort v49, v139, s[42:43]\n"
    "s_add_u32 s42, s42, 5408\n"
    "s_addc_u32 s43, s43, 0\n"
    "global_load_ushort v50, v139, s[42:43]\n"
    "s_add_u32 s42, s42, 5408\n"
    "s_addc_u32 s43, s43, 0\n"
    "global_load_ushort v51, v140, s[42:43]\n"
    "global_load_ushort v24, v4, s[14:15] offset:260\n"
    "global_load_ushort v25, v4, s[14:15] offset:268\n"
    "global_load_ushort v26, v4, s[14:15] offset:276\n"
    "global_load_ushort v27, v4, s[14:15] offset:284\n"
    "global_load_ushort v28, v4, s[14:15] offset:292\n"
    "global_load_ushort v29, v4, s[14:15] offset:300\n"
    "global_load_ushort v30, v5, s[14:15] offset:308\n"
    "s_sub_u32 s42, s42, 35152\n"
    "s_subb_u32 s43, s43, 0\n"
    "global_load_ushort v44, v140, s[42:43]\n"
    "s_sub_u32 s42, s42, 5408\n"
    "s_subb_u32 s43, s43, 0\n"
    "global_load_ushort v43, v139, s[42:43]\n"
    "s_sub_u32 s42, s42, 5408\n"
    "s_subb_u32 s43, s43, 0\n"
    "global_load_ushort v42, v139, s[42:43]\n"
    "s_sub_u32 s42, s42, 5408\n"
    "s_subb_u32 s43, s43, 0\n"
    "global_load_ushort v41, v139, s[42:43]\n"
    "s_sub_u32 s42, s42, 5408\n"
    "s_subb_u32 s43, s43, 0\n"
    "global_load_ushort v40, v139, s[42:43]\n"
    "s_sub_u32 s42, s42, 5408\n"
    "s_subb_u32 s43, s43, 0\n"
    "global_load_ushort v39, v139, s[42:43]\n"
    "s_sub_u32 s42, s42, 5408\n"
    "s_subb_u32 s43, s43, 0\n"
    "global_load_ushort v38, v139, s[42:43]\n"
    "global_load_ushort v31, v4, s[14:15] offset:208\n"
    "global_load_ushort v32, v4, s[14:15] offset:216\n"
    "global_load_ushort v33, v4, s[14:15] offset:224\n"
    "global_load_ushort v34, v4, s[14:15] offset:232\n"
    "global_load_ushort v35, v4, s[14:15] offset:240\n"
    "global_load_ushort v36, v4, s[14:15] offset:248\n"
    "global_load_ushort v37, v5, s[14:15] offset:256\n"
    "s_branch LS44_%=\n"
    "LS42_%=:\n"
    "v_mov_b32 v17, 153728\n"
    "v_mov_b32 v18, 1\n"
    "s_mov_b32 s28, 0\n"
    "v_min_u32 v32, 1, v5\n"
    "v_cvt_f32_u32 v32, v32\n"
    "v_min_u32 v33, 1, v6\n"
    "v_cvt_f32_u32 v33, v33\n"
    "s_mul_i32 s41, s2, 736\n"
    "s_add_u32 s41, s41, 36864\n"
    "v_lshl_add_u32 v9, v4, 4, s41\n"
    "v_add_u32 v10, 19136, v9\n"
    "v_cmp_ne_u32 s[44:45], 45, v4\n"
    "s_mul_i32 s40, s2, 1352\n"
    "s_add_u32 s40, s40, 70304\n"
    "v_subrev_u32 v64, 43, v1\n"
    "v_min_u32 v64, v1, v64\n"
    "v_sub_u32 v66, v1, v64\n"
    "v_and_b32 v66, 1, v66\n"
    "v_add_u32 v65, 21, v1\n"
    "v_min_u32 v65, 42, v65\n"
    "v_mov_b32 v67, 1\n"
    "v_mul_u32_u24 v68, 676, v66\n"
    "v_lshl_add_u32 v68, v64, 4, v68\n"
    "v_lshlrev_b32 v69, 4, v65\n"
    "s_add_u32 s46, s12, s40\n"
    "s_addc_u32 s47, s13, 0\n"
    "global_load_dwordx4 v[46:49], v68, s[46:47] offset:0\n"
    "global_load_dwordx4 v[50:53], v69, s[46:47] offset:676\n"
    "v_mad_u32_u24 v62, v66, 26, s2\n"
    "v_mul_u32_u24 v62, 688, v62\n"
    "v_lshl_add_u32 v62, v64, 4, v62\n"
    "v_cmp_ne_u32 s[48:49], 42, v64\n"
    "v_mad_u32_u24 v63, v67, 26, s2\n"
    "v_mul_u32_u24 v63, 688, v63\n"
    "v_lshl_add_u32 v63, v65, 4, v63\n"
    "v_cmp_ne_u32 s[50:51], 42, v65\n"
    "s_waitcnt lgkmcnt(0)\n"
    "s_add_u32 s56, s56, 2654435769\n"
    "v_mov_b32 v39, s56\n"
    "v_mov_b32 v41, s56\n"
    "v_mov_b32 v43, s56\n"
    "v_mov_b32 v45, s56\n"
    "s_waitcnt vmcnt(6)\n"
    "v_lshlrev_b32 v34, 16, v24\n"
    "v_and_b32 v35, 0xffff0000, v24\n"
    "v_lshlrev_b32 v36, 16, v28\n"
    "v_lshlrev_b32 v37, 16, v29\n"
    "v_fmac_f32 v34, v36, v32\n"
    "v_fmac_f32 v35, v37, v32\n"
    "v_cvt_pk_f16_f32 v38, v34, v35\n"
    "v_lshlrev_b32 v34, 16, v25\n"
    "v_and_b32 v35, 0xffff0000, v25\n"
    "v_and_b32 v36, 0xffff0000, v28\n"
    "v_and_b32 v37, 0xffff0000, v29\n"
    "v_fmac_f32 v34, v36, v32\n"
    "v_fmac_f32 v35, v37, v32\n"
    "v_cvt_pk_f16_f32 v42, v34, v35\n"
    "s_waitcnt vmcnt(2)\n"
    "v_lshlrev_b32 v34, 16, v26\n"
    "v_and_b32 v35, 0xffff0000, v26\n"
    "v_lshlrev_b32 v36, 16, v30\n"
    "v_lshlrev_b32 v37, 16, v31\n"
    "v_fmac_f32 v34, v36, v33\n"
    "v_fmac_f32 v35, v37, v33\n"
    "v_cvt_pk_f16_f32 v40, v34, v35\n"
    "v_cndmask_b32 v40, 0, v40, s[44:45]\n"
    "global_store_dwordx4 v9, v[38:41], s[26:27] sc1\n"
    "v_lshlrev_b32 v34, 16, v27\n"
    "v_and_b32 v35, 0xffff0000, v27\n"
    "v_and_b32 v36, 0xffff0000, v30\n"
    "v_and_b32 v37, 0xffff0000, v31\n"
    "v_fmac_f32 v34, v36, v33\n"
    "v_fmac_f32 v35, v37, v33\n"
    "v_cvt_pk_f16_f32 v44, v34, v35\n"
    "v_cndmask_b32 v44, 0, v44, s[44:45]\n"
    "global_store_dwordx4 v10, v[42:45], s[26:27] sc1\n"
    "s_setprio 3\n"
    "s_add_u32 s42, s26, 141312\n"
    "s_addc_u32 s43, s27, 0\n"
    "s_waitcnt vmcnt(3)\n"
    "v_lshlrev_b32 v34, 16, v46\n"
    "v_and_b32 v35, 0xffff0000, v46\n"
    "v_cvt_pk_f16_f32 v46, v34, v35\n"
    "v_lshlrev_b32 v34, 16, v47\n"
    "v_and_b32 v35, 0xffff0000, v47\n"
    "v_cvt_pk_f16_f32 v47, v34, v35\n"
    "v_cndmask_b32 v47, 0, v47, s[48:49]\n"
    "v_lshlrev_b32 v34, 16, v48\n"
    "v_and_b32 v35, 0xffff0000, v48\n"
    "v_cvt_pk_f16_f32 v48, v34, v35\n"
    "v_cndmask_b32 v48, 0, v48, s[48:49]\n"
    "v_lshlrev_b32 v34, 16, v49\n"
    "v_and_b32 v35, 0xffff0000, v49\n"
    "v_cvt_pk_f16_f32 v49, v34, v35\n"
    "v_cndmask_b32 v49, 0, v49, s[48:49]\n"
    "global_store_dwordx4 v62, v[46:49], s[42:43] sc1\n"
    "s_waitcnt vmcnt(3)\n"
    "v_lshlrev_b32 v34, 16, v50\n"
    "v_and_b32 v35, 0xffff0000, v50\n"
    "v_cvt_pk_f16_f32 v50, v34, v35\n"
    "v_lshlrev_b32 v34, 16, v51\n"
    "v_and_b32 v35, 0xffff0000, v51\n"
    "v_cvt_pk_f16_f32 v51, v34, v35\n"
    "v_cndmask_b32 v51, 0, v51, s[50:51]\n"
    "v_lshlrev_b32 v34, 16, v52\n"
    "v_and_b32 v35, 0xffff0000, v52\n"
    "v_cvt_pk_f16_f32 v52, v34, v35\n"
    "v_cndmask_b32 v52, 0, v52, s[50:51]\n"
    "v_lshlrev_b32 v34, 16, v53\n"
    "v_and_b32 v35, 0xffff0000, v53\n"
    "v_cvt_pk_f16_f32 v53, v34, v35\n"
    "v_cndmask_b32 v53, 0, v53, s[50:51]\n"
    "global_store_dwordx4 v63, v[50:53], s[42:43] sc1\n"
    "s_mov_b32 s40, 4\n"
    "v_or_b32 v8, v46, v47\n"
    "v_or3_b32 v8, v48, v49, v8\n"
    "v_or3_b32 v8, v50, v51, v8\n"
    "v_or3_b32 v8, v52, v53, v8\n"
    "v_and_b32 v8, 0x78007800, v8\n"
    "v_cmp_ne_u32 vcc, 0, v8\n"
    "s_nop 0\n"
    "s_cmp_lg_u64 vcc, 0\n"
    "s_cselect_b32 s41, 1, 0\n"
    "s_or_b32 s40, s40, s41\n"
    "v_or_b32 v8, v38, v40\n"
    "v_or3_b32 v8, v42, v44, v8\n"
    "v_and_b32 v8, 0x78007800, v8\n"
    "v_cmp_ne_u32 vcc, 0, v8\n"
    "s_nop 0\n"
    "s_cmp_lg_u64 vcc, 0\n"
    "s_cselect_b32 s41, 2, 0\n"
    "s_or_b32 s40, s40, s41\n"
    "v_mov_b32 v6, s40\n"
    "v_mov_b32 v7, s56\n"
    "s_lshl_b32 s41, s2, 7\n"
    "s_add_u32 s41, s41, 16384\n"
    "v_lshl_add_u32 v4, v1, 14, s41\n"
    "s_waitcnt vmcnt(0)\n"
    "s_mov_b64 exec, 0x3\n"
    "global_store_dwordx2 v4, v[6:7], s[26:27] sc1\n"
    "s_mov_b64 exec, -1\n"
    "s_branch LN8_%=\n"
    "LS43_%=:\n"
    "v_mov_b32 v17, 153728\n"
    "v_mov_b32 v18, 1\n"
    "s_mov_b32 s28, 0\n"
    "v_mad_u32_u24 v62, v66, 26, s2\n"
    "v_mul_u32_u24 v62, 688, v62\n"
    "v_lshl_add_u32 v62, v64, 4, v62\n"
    "v_cmp_ne_u32 s[48:49], 42, v64\n"
    "v_mad_u32_u24 v63, v67, 26, s2\n"
    "v_mul_u32_u24 v63, 688, v63\n"
    "v_lshl_add_u32 v63, v65, 4, v63\n"
    "v_cmp_ne_u32 s[50:51], 42, v65\n"
    "s_waitcnt lgkmcnt(0)\n"
    "s_add_u32 s56, s56, 2654435769\n"
    "s_add_u32 s42, s26, 82944\n"
    "s_addc_u32 s43, s27, 0\n"
    "s_waitcnt vmcnt(1)\n"
    "v_lshlrev_b32 v34, 16, v46\n"
    "v_and_b32 v35, 0xffff0000, v46\n"
    "v_cvt_pk_f16_f32 v46, v34, v35\n"
    "v_lshlrev_b32 v34, 16, v47\n"
    "v_and_b32 v35, 0xffff0000, v47\n"
    "v_cvt_pk_f16_f32 v47, v34, v35\n"
    "v_cndmask_b32 v47, 0, v47, s[48:49]\n"
    "v_lshlrev_b32 v34, 16, v48\n"
    "v_and_b32 v35, 0xffff0000, v48\n"
    "v_cvt_pk_f16_f32 v48, v34, v35\n"
    "v_cndmask_b32 v48, 0, v48, s[48:49]\n"
    "v_lshlrev_b32 v34, 16, v49\n"
    "v_and_b32 v35, 0xffff0000, v49\n"
    "v_cvt_pk_f16_f32 v49, v34, v35\n"
    "v_cndmask_b32 v49, 0, v49, s[48:49]\n"
    "global_store_dwordx4 v62, v[46:49], s[42:43] sc1\n"
    "s_waitcnt vmcnt(1)\n"
    "v_lshlrev_b32 v34, 16, v50\n"
    "v_and_b32 v35, 0xffff0000, v50\n"
    "v_cvt_pk_f16_f32 v50, v34, v35\n"
    "v_lshlrev_b32 v34, 16, v51\n"
    "v_and_b32 v35, 0xffff0000, v51\n"
    "v_cvt_pk_f16_f32 v51, v34, v35\n"
    "v_cndmask_b32 v51, 0, v51, s[50:51]\n"
    "v_lshlrev_b32 v34, 16, v52\n"
    "v_and_b32 v35, 0xffff0000, v52\n"
    "v_cvt_pk_f16_f32 v52, v34, v35\n"
    "v_cndmask_b32 v52, 0, v52, s[50:51]\n"
    "v_lshlrev_b32 v34, 16, v53\n"
    "v_and_b32 v35, 0xffff0000, v53\n"
    "v_cvt_pk_f16_f32 v53, v34, v35\n"
    "v_cndmask_b32 v53, 0, v53, s[50:51]\n"
    "global_store_dwordx4 v63, v[50:53], s[42:43] sc1\n"
    "s_mul_i32 s40, s2, 52\n"
    "s_add_u32 s40, s40, 105456\n"
    "s_add_u32 s46, s12, s40\n"
    "s_addc_u32 s47, s13, 0\n"
    "v_mul_u32_u24 v5, 4394, v66\n"
    "v_lshl_add_u32 v6, v64, 2, 0\n"
    "v_mul_u32_u24 v7, 79, v6\n"
    "v_lshrrev_b32 v7, 10, v7\n"
    "v_mul_u32_u24 v7, 325, v7\n"
    "v_add3_u32 v7, v7, v6, v5\n"
    "v_lshlrev_b32 v7, 2, v7\n"
    "global_load_dword v54, v7, s[46:47]\n"
    "v_lshl_add_u32 v6, v64, 2, 1\n"
    "v_min_u32 v6, 168, v6\n"
    "v_mul_u32_u24 v7, 79, v6\n"
    "v_lshrrev_b32 v7, 10, v7\n"
    "v_mul_u32_u24 v7, 325, v7\n"
    "v_add3_u32 v7, v7, v6, v5\n"
    "v_lshlrev_b32 v7, 2, v7\n"
    "global_load_dword v55, v7, s[46:47]\n"
    "v_lshl_add_u32 v6, v64, 2, 2\n"
    "v_min_u32 v6, 168, v6\n"
    "v_mul_u32_u24 v7, 79, v6\n"
    "v_lshrrev_b32 v7, 10, v7\n"
    "v_mul_u32_u24 v7, 325, v7\n"
    "v_add3_u32 v7, v7, v6, v5\n"
    "v_lshlrev_b32 v7, 2, v7\n"
    "global_load_dword v56, v7, s[46:47]\n"
    "v_lshl_add_u32 v6, v64, 2, 3\n"
    "v_min_u32 v6, 168, v6\n"
    "v_mul_u32_u24 v7, 79, v6\n"
    "v_lshrrev_b32 v7, 10, v7\n"
    "v_mul_u32_u24 v7, 325, v7\n"
    "v_add3_u32 v7, v7, v6, v5\n"
    "v_lshlrev_b32 v7, 2, v7\n"
    "global_load_dword v57, v7, s[46:47]\n"
    "v_mul_u32_u24 v5, 4394, v67\n"
    "v_lshl_add_u32 v6, v65, 2, 0\n"
    "v_mul_u32_u24 v7, 79, v6\n"
    "v_lshrrev_b32 v7, 10, v7\n"
    "v_mul_u32_u24 v7, 325, v7\n"
    "v_add3_u32 v7, v7, v6, v5\n"
    "v_lshlrev_b32 v7, 2, v7\n"
    "global_load_dword v58, v7, s[46:47]\n"
    "v_lshl_add_u32 v6, v65, 2, 1\n"
    "v_min_u32 v6, 168, v6\n"
    "v_mul_u32_u24 v7, 79, v6\n"
    "v_lshrrev_b32 v7, 10, v7\n"
    "v_mul_u32_u24 v7, 325, v7\n"
    "v_add3_u32 v7, v7, v6, v5\n"
    "v_lshlrev_b32 v7, 2, v7\n"
    "global_load_dword v59, v7, s[46:47]\n"
    "v_lshl_add_u32 v6, v65, 2, 2\n"
    "v_min_u32 v6, 168, v6\n"
    "v_mul_u32_u24 v7, 79, v6\n"
    "v_lshrrev_b32 v7, 10, v7\n"
    "v_mul_u32_u24 v7, 325, v7\n"
    "v_add3_u32 v7, v7, v6, v5\n"
    "v_lshlrev_b32 v7, 2, v7\n"
    "global_load_dword v60, v7, s[46:47]\n"
    "v_lshl_add_u32 v6, v65, 2, 3\n"
    "v_min_u32 v6, 168, v6\n"
    "v_mul_u32_u24 v7, 79, v6\n"
    "v_lshrrev_b32 v7, 10, v7\n"
    "v_mul_u32_u24 v7, 325, v7\n"
    "v_add3_u32 v7, v7, v6, v5\n"
    "v_lshlrev_b32 v7, 2, v7\n"
    "global_load_dword v61, v7, s[46:47]\n"
    "s_mov_b32 s40, 6\n"
    "v_or_b32 v8, v46, v47\n"
    "v_or3_b32 v8, v48, v49, v8\n"
    "v_or3_b32 v8, v50, v51, v8\n"
    "v_or3_b32 v8, v52, v53, v8\n"
    "v_and_b32 v8, 0x78007800, v8\n"
    "v_cmp_ne_u32 vcc, 0, v8\n"
    "s_nop 0\n"
    "s_cmp_lg_u64 vcc, 0\n"
    "s_cselect_b32 s41, 1, 0\n"
    "s_or_b32 s40, s40, s41\n"
    "v_mov_b32 v6, s40\n"
    "v_mov_b32 v7, s56\n"
    "s_lshl_b32 s41, s2, 7\n"
    "s_add_u32 s41, s41, 8192\n"
    "v_lshl_add_u32 v4, v1, 14, s41\n"
    "s_waitcnt vmcnt(8)\n"
    "s_mov_b64 exec, 0x3\n"
    "global_store_dwordx2 v4, v[6:7], s[26:27] sc1\n"
    "s_mov_b64 exec, -1\n"
    "s_setprio 3\n"
    "s_add_u32 s42, s26, 177152\n"
    "s_addc_u32 s43, s27, 0\n"
    "s_waitcnt vmcnt(5)\n"
    "v_lshlrev_b32 v34, 16, v54\n"
    "v_and_b32 v35, 0xffff0000, v54\n"
    "v_cvt_pk_f16_f32 v54, v34, v35\n"
    "v_lshlrev_b32 v34, 16, v55\n"
    "v_and_b32 v35, 0xffff0000, v55\n"
    "v_cvt_pk_f16_f32 v55, v34, v35\n"
    "v_cndmask_b32 v55, 0, v55, s[48:49]\n"
    "v_lshlrev_b32 v34, 16, v56\n"
    "v_and_b32 v35, 0xffff0000, v56\n"
    "v_cvt_pk_f16_f32 v56, v34, v35\n"
    "v_cndmask_b32 v56, 0, v56, s[48:49]\n"
    "v_lshlrev_b32 v34, 16, v57\n"
    "v_and_b32 v35, 0xffff0000, v57\n"
    "v_cvt_pk_f16_f32 v57, v34, v35\n"
    "v_cndmask_b32 v57, 0, v57, s[48:49]\n"
    "global_store_dwordx4 v62, v[54:57], s[42:43] sc1\n"
    "s_waitcnt vmcnt(2)\n"
    "v_lshlrev_b32 v34, 16, v58\n"
    "v_and_b32 v35, 0xffff0000, v58\n"
    "v_cvt_pk_f16_f32 v58, v34, v35\n"
    "v_lshlrev_b32 v34, 16, v59\n"
    "v_and_b32 v35, 0xffff0000, v59\n"
    "v_cvt_pk_f16_f32 v59, v34, v35\n"
    "v_cndmask_b32 v59, 0, v59, s[50:51]\n"
    "v_lshlrev_b32 v34, 16, v60\n"
    "v_and_b32 v35, 0xffff0000, v60\n"
    "v_cvt_pk_f16_f32 v60, v34, v35\n"
    "v_cndmask_b32 v60, 0, v60, s[50:51]\n"
    "v_lshlrev_b32 v34, 16, v61\n"
    "v_and_b32 v35, 0xffff0000, v61\n"
    "v_cvt_pk_f16_f32 v61, v34, v35\n"
    "v_cndmask_b32 v61, 0, v61, s[50:51]\n"
    "global_store_dwordx4 v63, v[58:61], s[42:43] sc1\n"
    "s_mov_b32 s40, 6\n"
    "v_or_b32 v8, v54, v55\n"
    "v_or3_b32 v8, v56, v57, v8\n"
    "v_or3_b32 v8, v58, v59, v8\n"
    "v_or3_b32 v8, v60, v61, v8\n"
    "v_and_b32 v8, 0x78007800, v8\n"
    "v_cmp_ne_u32 vcc, 0, v8\n"
    "s_nop 0\n"
    "s_cmp_lg_u64 vcc, 0\n"
    "s_cselect_b32 s41, 1, 0\n"
    "s_or_b32 s40, s40, s41\n"
    "v_mov_b32 v6, s40\n"
    "v_mov_b32 v7, s56\n"
    "s_lshl_b32 s41, s2, 7\n"
    "s_add_u32 s41, s41, 4096\n"
    "v_lshl_add_u32 v4, v1, 14, s41\n"
    "s_waitcnt vmcnt(0)\n"
    "s_mov_b64 exec, 0x3\n"
    "global_store_dwordx2 v4, v[6:7], s[26:27] sc1\n"
    "s_mov_b64 exec, -1\n"
    "s_branch LN8_%=\n"
    ".p2align 12\n"
    "LS34_%=:\n"
    "v_mov_b32 v17, 153728\n"
    "v_mov_b32 v18, 1\n"
    "s_mov_b32 s28, 0\n"
    "s_setprio 3\n"
    "v_lshrrev_b32 v5, 5, v1\n"
    "v_and_b32 v4, 31, v1\n"
    "v_mul_u32_u24 v6, 104, v4\n"
    "v_mad_u32_u24 v6, v5, 52, v6\n"
    "v_add_u32 v6, s37, v6\n"
    "v_add_u32 v6, 19456, v6\n"
    "s_add_u32 s44, s44, 153088\n"
    "v_lshl_add_u32 v10, v1, 2, s44\n"
    "v_bfe_u32 v7, v1, 2, 2\n"
    "v_lshlrev_b32 v226, 7, v7\n"
    "v_bfe_u32 v7, v1, 3, 1\n"
    "v_lshl_add_u32 v226, v7, 6, v226\n"
    "v_bfe_u32 v7, v1, 4, 1\n"
    "v_lshl_add_u32 v226, v7, 5, v226\n"
    "v_and_b32 v8, 3, v1\n"
    "v_lshl_add_u32 v226, v8, 3, v226\n"
    "v_mul_u32_u24 v8, 26624, v5\n"
    "v_add_u32 v226, v226, v8\n"
    "v_add_u32 v226, 89088, v226\n"
    "v_xor_b32 v227, 64, v226\n"
    "v_lshlrev_b32 v228, 4, v5\n"
    "v_add_u32 v228, 153088, v228\n"
    "s_lshl_b32 s41, s29, 11\n"
    "s_lshl_b32 s43, s57, 7\n"
    "s_add_u32 s41, s41, s43\n"
    "s_add_u32 s41, s41, 262144\n"
    "v_and_b32 v8, 15, v1\n"
    "v_lshlrev_b32 v229, 3, v8\n"
    "v_lshl_add_u32 v229, v7, 10, v229\n"
    "v_add_u32 v229, s41, v229\n"
    "v_mov_b32 v22, 0xbdd2d3e8\n"
    "v_mov_b32 v21, 0\n"
    "v_mov_b32 v11, 56320\n"
    "s_waitcnt lgkmcnt(0)\n"
    "s_add_u32 s56, s56, 2654435769\n"
    "v_mov_b32 v231, s56\n"
    "s_waitcnt vmcnt(8)\n"
    "ds_read2_b32 v[24:25], v6 offset0:0 offset1:1\n"
    "ds_read2_b32 v[26:27], v6 offset0:2 offset1:3\n"
    "ds_read2_b32 v[28:29], v6 offset0:4 offset1:5\n"
    "ds_read2_b32 v[30:31], v6 offset0:6 offset1:7\n"
    "ds_read2_b32 v[32:33], v6 offset0:8 offset1:9\n"
    "ds_read2_b32 v[34:35], v6 offset0:10 offset1:11\n"
    "ds_read_b32 v36, v6 offset:48\n"
    "s_waitcnt vmcnt(7)\n"
    "v_lshlrev_b32 v23, 16, v23\n"
    "v_cndmask_b32 v23, 0, v23, s[50:51]\n"
    "ds_write_b32 v10, v23\n"
    "s_waitcnt lgkmcnt(7)\n"
    "v_and_b32 v8, 8, v24\n"
    "v_lshl_add_u32 v8, v8, 1, v11\n"
    "v_lshl_add_u32 v24, v24, 5, v8\n"
    "v_xor_b32 v202, 16, v24\n"
    "v_and_b32 v8, 8, v25\n"
    "v_lshl_add_u32 v8, v8, 1, v11\n"
    "v_lshl_add_u32 v25, v25, 5, v8\n"
    "v_xor_b32 v203, 16, v25\n"
    "s_waitcnt lgkmcnt(6)\n"
    "v_and_b32 v8, 8, v26\n"
    "v_lshl_add_u32 v8, v8, 1, v11\n"
    "v_lshl_add_u32 v26, v26, 5, v8\n"
    "v_xor_b32 v204, 16, v26\n"
    "v_and_b32 v8, 8, v27\n"
    "v_lshl_add_u32 v8, v8, 1, v11\n"
    "v_lshl_add_u32 v27, v27, 5, v8\n"
    "v_xor_b32 v205, 16, v27\n"
    "L35_%=:\n"
    "ds_read_b32 v19, v17 offset:36\n"
    "s_waitcnt lgkmcnt(0)\n"
    "v_readfirstlane_b32 s55, v19\n"
    "s_nop 0\n"
    "s_cmp_ge_u32 s55, 4\n"
    "s_cbranch_scc1 L36_%=\n"
    "s_branch L35_%=\n"
    "L36_%=:\n"
    "ds_read_b128 v[38:41], v24\n"
    "ds_read_b128 v[42:45], v202\n"
    "ds_read_b128 v[46:49], v25\n"
    "ds_read_b128 v[50:53], v203\n"
    "ds_read_b128 v[54:57], v26\n"
    "ds_read_b128 v[58:61], v204\n"
    "ds_read_b128 v[62:65], v27\n"
    "ds_read_b128 v[66:69], v205\n"
    "v_and_b32 v8, 8, v28\n"
    "v_lshl_add_u32 v8, v8, 1, v11\n"
    "v_lshl_add_u32 v28, v28, 5, v8\n"
    "v_xor_b32 v206, 16, v28\n"
    "ds_read_b128 v[70:73], v28\n"
    "ds_read_b128 v[74:77], v206\n"
    "v_and_b32 v8, 8, v29\n"
    "v_lshl_add_u32 v8, v8, 1, v11\n"
    "v_lshl_add_u32 v29, v29, 5, v8\n"
    "v_xor_b32 v207, 16, v29\n"
    "ds_read_b128 v[78:81], v29\n"
    "ds_read_b128 v[82:85], v207\n"
    "v_and_b32 v8, 8, v30\n"
    "v_lshl_add_u32 v8, v8, 1, v11\n"
    "v_lshl_add_u32 v30, v30, 5, v8\n"
    "v_xor_b32 v208, 16, v30\n"
    "ds_read_b128 v[86:89], v30\n"
    "ds_read_b128 v[90:93], v208\n"
    "v_and_b32 v8, 8, v31\n"
    "v_lshl_add_u32 v8, v8, 1, v11\n"
    "v_lshl_add_u32 v31, v31, 5, v8\n"
    "v_xor_b32 v209, 16, v31\n"
    "ds_read_b128 v[94:97], v31\n"
    "ds_read_b128 v[98:101], v209\n"
    "global_load_lds_dwordx4 v219, s[46:47] offset:2048\n"
    "s_add_u32 m0, s36, 105472\n"
    "s_add_u32 s46, s46, 26624\n"
    "s_addc_u32 s47, s47, 0\n"
    "global_load_lds_dwordx4 v218, s[46:47] offset:0\n"
    "global_load_lds_dwordx4 v219, s[46:47] offset:2048\n"
    "s_add_u32 m0, s36, 109568\n"
    "s_add_u32 s46, s46, 26624\n"
    "s_addc_u32 s47, s47, 0\n"
    "global_load_lds_dwordx4 v218, s[46:47] offset:0\n"
    "global_load_lds_dwordx4 v219, s[46:47] offset:2048\n"
    "s_add_u32 m0, s36, 113664\n"
    "s_add_u32 s46, s46, 26624\n"
    "s_addc_u32 s47, s47, 0\n"
    "global_load_lds_dwordx4 v218, s[46:47] offset:0\n"
    "s_add_u32 s64, s26, 63488\n"
    "s_addc_u32 s65, s27, 0\n"
    "v_min_u32 v10, 25, v1\n"
    "s_and_b32 s44, s2, 1\n"
    "s_lshl_b32 s44, s44, 14\n"
    "s_add_u32 s44, s44, 4096\n"
    "v_lshl_add_u32 v10, v10, 7, s44\n"
    "v_add_u32 v4, 4096, v10\n"
    "s_mul_i32 s52, s31, 9216\n"
    "s_min_u32 s42, s52, 26624\n"
    "s_add_u32 s52, s52, 19456\n"
    "s_add_u32 s42, s42, 113664\n"
    "s_and_b32 s43, s31, 1\n"
    "s_mul_i32 s44, s43, 17408\n"
    "s_mul_i32 s40, s43, 10240\n"
    "s_add_u32 s40, s40, 56320\n"
    "s_add_u32 s44, s44, 77824\n"
    "s_cmp_ge_u32 s31, 2\n"
    "s_cselect_b32 s40, s40, s44\n"
    "s_cselect_b32 s41, 15, 0xff\n"
    "s_cselect_b32 s44, 24, 16\n"
    "s_cselect_b32 s43, 2, 3\n"
    "v_add_u32 v9, 8, v17\n"
    "v_add_u32 v12, s44, v17\n"
    "v_lshl_add_u32 v5, s43, 12, v10\n"
    "s_cselect_b32 s43, 11, 18\n"
    "s_mov_b32 s54, 9\n"
    "s_mov_b32 s53, 0\n"
    "s_mov_b32 s45, 65536\n"
    "s_mov_b32 s59, 0\n"
    "s_waitcnt vmcnt(6)\n"
    "s_mov_b64 exec, 1\n"
    "ds_add_u32 v17, v18 offset:28\n"
    "s_mov_b64 exec, -1\n"
    "L37_%=:\n"
    "ds_read_b32 v19, v17 offset:28\n"
    "s_waitcnt lgkmcnt(0)\n"
    "v_readfirstlane_b32 s55, v19\n"
    "s_nop 0\n"
    "s_cmp_ge_u32 s55, 4\n"
    "s_cbranch_scc1 L38_%=\n"
    "s_branch L37_%=\n"
    "L38_%=:\n"
    "ds_read_b128 v[170:173], v228 offset:0\n"
    "ds_read_b128 v[174:177], v228 offset:32\n"
    "ds_read_b128 v[178:181], v228 offset:64\n"
    "ds_read_b128 v[182:185], v228 offset:96\n"
    "ds_read_b128 v[186:189], v228 offset:128\n"
    "ds_read_b128 v[190:193], v228 offset:160\n"
    "ds_read_b128 v[194:197], v228 offset:192\n"
    "ds_read_b128 v[198:201], v228 offset:224\n"
    "ds_read_b64_tr_b16 v[142:143], v226 offset:0\n"
    "ds_read_b64_tr_b16 v[144:145], v226 offset:512\n"
    "ds_read_b64_tr_b16 v[146:147], v227 offset:0\n"
    "ds_read_b64_tr_b16 v[148:149], v227 offset:512\n"
    "ds_read_b64_tr_b16 v[150:151], v226 offset:1024\n"
    "ds_read_b64_tr_b16 v[152:153], v226 offset:1536\n"
    "ds_read_b64_tr_b16 v[154:155], v227 offset:1024\n"
    "ds_read_b64_tr_b16 v[156:157], v227 offset:1536\n"
    "ds_read_b64_tr_b16 v[158:159], v226 offset:2048\n"
    "ds_read_b64_tr_b16 v[160:161], v226 offset:2560\n"
    "ds_read_b64_tr_b16 v[162:163], v227 offset:2048\n"
    "ds_read_b64_tr_b16 v[164:165], v227 offset:2560\n"
    "v_and_b32 v8, 8, v32\n"
    "v_lshl_add_u32 v8, v8, 1, v11\n"
    "v_lshl_add_u32 v32, v32, 5, v8\n"
    "v_xor_b32 v210, 16, v32\n"
    "ds_read_b128 v[102:105], v32\n"
    "s_waitcnt lgkmcnt(11)\n"
    "v_mfma_f32_32x32x16_bf16 v[170:185], v[142:145], v[38:41], v[170:185]\n"
    "ds_read_b64_tr_b16 v[166:167], v226 offset:3072\n"
    "ds_read_b64_tr_b16 v[168:169], v226 offset:3584\n"
    "s_waitcnt lgkmcnt(11)\n"
    "v_mfma_f32_32x32x16_bf16 v[186:201], v[146:149], v[38:41], v[186:201]\n"
    "ds_read_b64_tr_b16 v[142:143], v227 offset:3072\n"
    "ds_read_b64_tr_b16 v[144:145], v227 offset:3584\n"
    "ds_read_b128 v[106:109], v210\n"
    "s_waitcnt lgkmcnt(12)\n"
    "v_mfma_f32_32x32x16_bf16 v[170:185], v[150:153], v[42:45], v[170:185]\n"
    "ds_read_b64_tr_b16 v[146:147], v226 offset:4096\n"
    "ds_read_b64_tr_b16 v[148:149], v226 offset:4608\n"
    "s_waitcnt lgkmcnt(12)\n"
    "v_mfma_f32_32x32x16_bf16 v[186:201], v[154:157], v[42:45], v[186:201]\n"
    "ds_read_b64_tr_b16 v[150:151], v227 offset:4096\n"
    "ds_read_b64_tr_b16 v[152:153], v227 offset:4608\n"
    "v_and_b32 v8, 8, v33\n"
    "v_lshl_add_u32 v8, v8, 1, v11\n"
    "v_lshl_add_u32 v33, v33, 5, v8\n"
    "v_xor_b32 v211, 16, v33\n"
    "ds_read_b128 v[110:113], v33\n"
    "s_waitcnt lgkmcnt(13)\n"
    "v_mfma_f32_32x32x16_bf16 v[170:185], v[158:161], v[46:49], v[170:185]\n"
    "ds_read_b64_tr_b16 v[154:155], v226 offset:5120\n"
    "ds_read_b64_tr_b16 v[156:157], v226 offset:5632\n"
    "s_waitcnt lgkmcnt(13)\n"
    "v_mfma_f32_32x32x16_bf16 v[186:201], v[162:165], v[46:49], v[186:201]\n"
    "ds_read_b64_tr_b16 v[158:159], v227 offset:5120\n"
    "ds_read_b64_tr_b16 v[160:161], v227 offset:5632\n"
    "ds_read_b128 v[114:117], v211\n"
    "s_waitcnt lgkmcnt(13)\n"
    "v_mfma_f32_32x32x16_bf16 v[170:185], v[166:169], v[50:53], v[170:185]\n"
    "ds_read_b64_tr_b16 v[162:163], v226 offset:6144\n"
    "ds_read_b64_tr_b16 v[164:165], v226 offset:6656\n"
    "s_waitcnt lgkmcnt(13)\n"
    "v_mfma_f32_32x32x16_bf16 v[186:201], v[142:145], v[50:53], v[186:201]\n"
    "ds_read_b64_tr_b16 v[166:167], v227 offset:6144\n"
    "ds_read_b64_tr_b16 v[168:169], v227 offset:6656\n"
    "v_and_b32 v8, 8, v34\n"
    "v_lshl_add_u32 v8, v8, 1, v11\n"
    "v_lshl_add_u32 v34, v34, 5, v8\n"
    "v_xor_b32 v212, 16, v34\n"
    "ds_read_b128 v[118:121], v34\n"
    "s_waitcnt lgkmcnt(13)\n"
    "v_mfma_f32_32x32x16_bf16 v[170:185], v[146:149], v[54:57], v[170:185]\n"
    "ds_read_b64_tr_b16 v[142:143], v226 offset:7168\n"
    "ds_read_b64_tr_b16 v[144:145], v226 offset:7680\n"
    "s_waitcnt lgkmcnt(13)\n"
    "v_mfma_f32_32x32x16_bf16 v[186:201], v[150:153], v[54:57], v[186:201]\n"
    "ds_read_b64_tr_b16 v[146:147], v227 offset:7168\n"
    "ds_read_b64_tr_b16 v[148:149], v227 offset:7680\n"
    "ds_read_b128 v[122:125], v212\n"
    "s_waitcnt lgkmcnt(13)\n"
    "v_mfma_f32_32x32x16_bf16 v[170:185], v[154:157], v[58:61], v[170:185]\n"
    "ds_read_b64_tr_b16 v[150:151], v226 offset:8192\n"
    "ds_read_b64_tr_b16 v[152:153], v226 offset:8704\n"
    "s_waitcnt lgkmcnt(13)\n"
    "v_mfma_f32_32x32x16_bf16 v[186:201], v[158:161], v[58:61], v[186:201]\n"
    "ds_read_b64_tr_b16 v[154:155], v227 offset:8192\n"
    "ds_read_b64_tr_b16 v[156:157], v227 offset:8704\n"
    "v_and_b32 v8, 8, v35\n"
    "v_lshl_add_u32 v8, v8, 1, v11\n"
    "v_lshl_add_u32 v35, v35, 5, v8\n"
    "v_xor_b32 v213, 16, v35\n"
    "ds_read_b128 v[126:129], v35\n"
    "s_waitcnt lgkmcnt(13)\n"
    "v_mfma_f32_32x32x16_bf16 v[170:185], v[162:165], v[62:65], v[170:185]\n"
    "ds_read_b64_tr_b16 v[158:159], v226 offset:9216\n"
    "ds_read_b64_tr_b16 v[160:161], v226 offset:9728\n"
    "s_waitcnt lgkmcnt(13)\n"
    "v_mfma_f32_32x32x16_bf16 v[186:201], v[166:169], v[62:65], v[186:201]\n"
    "ds_read_b64_tr_b16 v[162:163], v227 offset:9216\n"
    "ds_read_b64_tr_b16 v[164:165], v227 offset:9728\n"
    "ds_read_b128 v[130:133], v213\n"
    "s_waitcnt lgkmcnt(13)\n"
    "v_mfma_f32_32x32x16_bf16 v[170:185], v[142:145], v[66:69], v[170:185]\n"
    "s_waitcnt vmcnt(0)\n"
    "s_mov_b64 exec, 1\n"
    "ds_add_u32 v17, v18 offset:32\n"
    "s_mov_b64 exec, -1\n"
    "ds_read_b64_tr_b16 v[166:167], v226 offset:10240\n"
    "ds_read_b64_tr_b16 v[168:169], v226 offset:10752\n"
    "s_waitcnt lgkmcnt(14)\n"
    "v_mfma_f32_32x32x16_bf16 v[186:201], v[146:149], v[66:69], v[186:201]\n"
    "ds_read_b64_tr_b16 v[142:143], v227 offset:10240\n"
    "ds_read_b64_tr_b16 v[144:145], v227 offset:10752\n"
    "v_and_b32 v8, 8, v36\n"
    "v_lshl_add_u32 v8, v8, 1, v11\n"
    "v_lshl_add_u32 v36, v36, 5, v8\n"
    "v_xor_b32 v214, 16, v36\n"
    "ds_read_b128 v[134:137], v36\n"
    "s_waitcnt lgkmcnt(14)\n"
    "v_mfma_f32_32x32x16_bf16 v[170:185], v[150:153], v[70:73], v[170:185]\n"
    "ds_read_b64_tr_b16 v[146:147], v226 offset:11264\n"
    "ds_read_b64_tr_b16 v[148:149], v226 offset:11776\n"
    "s_waitcnt lgkmcnt(14)\n"
    "v_mfma_f32_32x32x16_bf16 v[186:201], v[154:157], v[70:73], v[186:201]\n"
    "ds_read_b64_tr_b16 v[150:151], v227 offset:11264\n"
    "ds_read_b64_tr_b16 v[152:153], v227 offset:11776\n"
    "ds_read_b128 v[138:141], v214\n"
    "s_mov_b64 exec, 1\n"
    "ds_add_u32 v17, v18 offset:12\n"
    "s_mov_b64 exec, -1\n"
    "s_waitcnt lgkmcnt(15)\n"
    "v_mfma_f32_32x32x16_bf16 v[170:185], v[158:161], v[74:77], v[170:185]\n"
    "ds_read_b64_tr_b16 v[154:155], v226 offset:12288\n"
    "ds_read_b64_tr_b16 v[156:157], v226 offset:12800\n"
    "s_waitcnt lgkmcnt(15)\n"
    "v_mfma_f32_32x32x16_bf16 v[186:201], v[162:165], v[74:77], v[186:201]\n"
    "ds_read_b32 v19, v17 offset:32\n"
    "ds_read_b64_tr_b16 v[158:159], v227 offset:12288\n"
    "ds_read_b64_tr_b16 v[160:161], v227 offset:12800\n"
    "ds_read_b128 v[202:205], v228 offset:256\n"
    "s_waitcnt lgkmcnt(15)\n"
    "v_mfma_f32_32x32x16_bf16 v[170:185], v[166:169], v[78:81], v[170:185]\n"
    "ds_read_b64_tr_b16 v[162:163], v226 offset:13312\n"
    "ds_read_b64_tr_b16 v[164:165], v226 offset:13824\n"
    "ds_read_b128 v[206:209], v228 offset:288\n"
    "s_waitcnt lgkmcnt(15)\n"
    "v_mfma_f32_32x32x16_bf16 v[186:201], v[142:145], v[78:81], v[186:201]\n"
    "ds_read_b64_tr_b16 v[166:167], v227 offset:13312\n"
    "ds_read_b64_tr_b16 v[168:169], v227 offset:13824\n"
    "ds_read_b128 v[210:213], v228 offset:320\n"
    "s_waitcnt lgkmcnt(15)\n"
    "v_mfma_f32_32x32x16_bf16 v[170:185], v[146:149], v[82:85], v[170:185]\n"
    "s_waitcnt lgkmcnt(9)\n"
    "v_cmp_gt_u32 vcc, 4, v19\n"
    "s_nop 0\n"
    "s_cbranch_vccnz L40_%=\n"
    "L39_%=:\n"
    "ds_read_b64_tr_b16 v[142:143], v226 offset:14336\n"
    "ds_read_b64_tr_b16 v[144:145], v226 offset:14848\n"
    "ds_read_b128 v[214:217], v228 offset:352\n"
    "v_mfma_f32_32x32x16_bf16 v[186:201], v[150:153], v[82:85], v[186:201]\n"
    "ds_read_b64_tr_b16 v[146:147], v227 offset:14336\n"
    "ds_read_b64_tr_b16 v[148:149], v227 offset:14848\n"
    "v_mfma_f32_32x32x16_bf16 v[170:185], v[154:157], v[86:89], v[170:185]\n"
    "ds_read_b64_tr_b16 v[150:151], v226 offset:15360\n"
    "ds_read_b64_tr_b16 v[152:153], v226 offset:15872\n"
    "s_waitcnt lgkmcnt(14)\n"
    "v_mfma_f32_32x32x16_bf16 v[186:201], v[158:161], v[86:89], v[186:201]\n"
    "ds_read_b64_tr_b16 v[154:155], v227 offset:15360\n"
    "ds_read_b64_tr_b16 v[156:157], v227 offset:15872\n"
    "s_waitcnt lgkmcnt(13)\n"
    "v_mfma_f32_32x32x16_bf16 v[170:185], v[162:165], v[90:93], v[170:185]\n"
    "ds_read_b64_tr_b16 v[158:159], v226 offset:16384\n"
    "ds_read_b64_tr_b16 v[160:161], v226 offset:16896\n"
    "s_waitcnt lgkmcnt(12)\n"
    "v_mfma_f32_32x32x16_bf16 v[186:201], v[166:169], v[90:93], v[186:201]\n"
    "ds_read_b64_tr_b16 v[162:163], v227 offset:16384\n"
    "ds_read_b64_tr_b16 v[164:165], v227 offset:16896\n"
    "s_waitcnt lgkmcnt(11)\n"
    "v_mfma_f32_32x32x16_bf16 v[170:185], v[142:145], v[94:97], v[170:185]\n"
    "ds_read_b64_tr_b16 v[166:167], v226 offset:17408\n"
    "ds_read_b64_tr_b16 v[168:169], v226 offset:17920\n"
    "s_waitcnt lgkmcnt(10)\n"
    "v_mfma_f32_32x32x16_bf16 v[186:201], v[146:149], v[94:97], v[186:201]\n"
    "ds_read_b64_tr_b16 v[142:143], v227 offset:17408\n"
    "ds_read_b64_tr_b16 v[144:145], v227 offset:17920\n"
    "s_waitcnt lgkmcnt(10)\n"
    "v_mfma_f32_32x32x16_bf16 v[170:185], v[150:153], v[98:101], v[170:185]\n"
    "ds_read_b64_tr_b16 v[146:147], v226 offset:18432\n"
    "ds_read_b64_tr_b16 v[148:149], v226 offset:18944\n"
    "s_waitcnt lgkmcnt(10)\n"
    "v_mfma_f32_32x32x16_bf16 v[186:201], v[154:157], v[98:101], v[186:201]\n"
    "ds_read_b64_tr_b16 v[150:151], v227 offset:18432\n"
    "ds_read_b64_tr_b16 v[152:153], v227 offset:18944\n"
    "s_waitcnt lgkmcnt(10)\n"
    "v_mfma_f32_32x32x16_bf16 v[170:185], v[158:161], v[102:105], v[170:185]\n"
    "ds_read_b64_tr_b16 v[154:155], v226 offset:19456\n"
    "ds_read_b64_tr_b16 v[156:157], v226 offset:19968\n"
    "s_waitcnt lgkmcnt(10)\n"
    "v_mfma_f32_32x32x16_bf16 v[186:201], v[162:165], v[102:105], v[186:201]\n"
    "ds_read_b64_tr_b16 v[158:159], v227 offset:19456\n"
    "ds_read_b64_tr_b16 v[160:161], v227 offset:19968\n"
    "s_waitcnt lgkmcnt(10)\n"
    "v_mfma_f32_32x32x16_bf16 v[170:185], v[166:169], v[106:109], v[170:185]\n"
    "ds_read_b64_tr_b16 v[162:163], v226 offset:20480\n"
    "ds_read_b64_tr_b16 v[164:165], v226 offset:20992\n"
    "s_waitcnt lgkmcnt(10)\n"
    "v_mfma_f32_32x32x16_bf16 v[186:201], v[142:145], v[106:109], v[186:201]\n"
    "ds_read_b64_tr_b16 v[166:167], v227 offset:20480\n"
    "ds_read_b64_tr_b16 v[168:169], v227 offset:20992\n"
    "s_waitcnt lgkmcnt(10)\n"
    "v_mfma_f32_32x32x16_bf16 v[170:185], v[146:149], v[110:113], v[170:185]\n"
    "global_load_dwordx2 v[6:7], v4, s[26:27] sc1\n"
    "ds_read_b64_tr_b16 v[142:143], v226 offset:21504\n"
    "ds_read_b64_tr_b16 v[144:145], v226 offset:22016\n"
    "s_waitcnt lgkmcnt(10)\n"
    "v_mfma_f32_32x32x16_bf16 v[186:201], v[150:153], v[110:113], v[186:201]\n"
    "ds_read_b64_tr_b16 v[146:147], v227 offset:21504\n"
    "ds_read_b64_tr_b16 v[148:149], v227 offset:22016\n"
    "s_waitcnt lgkmcnt(10)\n"
    "v_mfma_f32_32x32x16_bf16 v[170:185], v[154:157], v[114:117], v[170:185]\n"
    "ds_read_b64_tr_b16 v[150:151], v226 offset:22528\n"
    "ds_read_b64_tr_b16 v[152:153], v226 offset:23040\n"
    "s_waitcnt lgkmcnt(10)\n"
    "v_mfma_f32_32x32x16_bf16 v[186:201], v[158:161], v[114:117], v[186:201]\n"
    "ds_read_b64_tr_b16 v[154:155], v227 offset:22528\n"
    "ds_read_b64_tr_b16 v[156:157], v227 offset:23040\n"
    "s_waitcnt lgkmcnt(10)\n"
    "v_mfma_f32_32x32x16_bf16 v[170:185], v[162:165], v[118:121], v[170:185]\n"
    "ds_read_b64_tr_b16 v[158:159], v226 offset:23552\n"
    "ds_read_b64_tr_b16 v[160:161], v226 offset:24064\n"
    "s_waitcnt lgkmcnt(10)\n"
    "v_mfma_f32_32x32x16_bf16 v[186:201], v[166:169], v[118:121], v[186:201]\n"
    "ds_read_b64_tr_b16 v[162:163], v227 offset:23552\n"
    "ds_read_b64_tr_b16 v[164:165], v227 offset:24064\n"
    "s_waitcnt lgkmcnt(10)\n"
    "v_mfma_f32_32x32x16_bf16 v[170:185], v[142:145], v[122:125], v[170:185]\n"
    "ds_read_b64_tr_b16 v[166:167], v226 offset:24576\n"
    "ds_read_b64_tr_b16 v[168:169], v226 offset:25088\n"
    "s_waitcnt lgkmcnt(10)\n"
    "v_mfma_f32_32x32x16_bf16 v[186:201], v[146:149], v[122:125], v[186:201]\n"
    "ds_read_b64_tr_b16 v[142:143], v227 offset:24576\n"
    "ds_read_b64_tr_b16 v[144:145], v227 offset:25088\n"
    "s_waitcnt lgkmcnt(10)\n"
    "v_mfma_f32_32x32x16_bf16 v[170:185], v[150:153], v[126:129], v[170:185]\n"
    "ds_read_b64_tr_b16 v[146:147], v226 offset:25600\n"
    "ds_read_b64_tr_b16 v[148:149], v226 offset:26112\n"
    "s_waitcnt lgkmcnt(10)\n"
    "v_mfma_f32_32x32x16_bf16 v[186:201], v[154:157], v[126:129], v[186:201]\n"
    "ds_read_b64_tr_b16 v[150:151], v227 offset:25600\n"
    "ds_read_b64_tr_b16 v[152:153], v227 offset:26112\n"
    "s_waitcnt lgkmcnt(10)\n"
    "v_mfma_f32_32x32x16_bf16 v[170:185], v[158:161], v[130:133], v[170:185]\n"
    "s_waitcnt lgkmcnt(8)\n"
    "v_mfma_f32_32x32x16_bf16 v[186:201], v[162:165], v[130:133], v[186:201]\n"
    "s_waitcnt lgkmcnt(6)\n"
    "v_mfma_f32_32x32x16_bf16 v[170:185], v[166:169], v[134:137], v[170:185]\n"
    "s_waitcnt lgkmcnt(4)\n"
    "v_mfma_f32_32x32x16_bf16 v[186:201], v[142:145], v[134:137], v[186:201]\n"
    "s_waitcnt lgkmcnt(2)\n"
    "v_mfma_f32_32x32x16_bf16 v[170:185], v[146:149], v[138:141], v[170:185]\n"
    "s_waitcnt lgkmcnt(0)\n"
    "v_mfma_f32_32x32x16_bf16 v[186:201], v[150:153], v[138:141], v[186:201]\n"
    "ds_read_b128 v[38:41], v228 offset:384\n"
    "ds_read_b128 v[42:45], v228 offset:416\n"
    "ds_read_b128 v[46:49], v228 offset:448\n"
    "ds_read_b128 v[50:53], v228 offset:480\n"
    "v_mov_b32 v13, 16\n"
    "s_mov_b64 exec, 1\n"
    "ds_add_u32 v17, v13 offset:12\n"
    "s_mov_b64 exec, -1\n"
    "s_call_b64 s[50:51], LT33_%=\n"
    "s_nop 0\n"
    "v_mul_f32 v218, v170, v170\n"
    "v_mul_f32 v219, v171, v171\n"
    "v_mul_f32 v220, v172, v172\n"
    "v_mul_f32 v221, v173, v173\n"
    "v_fmaak_f32 v218, v218, v22, 0xc0135761\n"
    "v_fmaak_f32 v219, v219, v22, 0xc0135761\n"
    "v_fmaak_f32 v220, v220, v22, 0xc0135761\n"
    "v_fmaak_f32 v221, v221, v22, 0xc0135761\n"
    "v_mul_f32 v218, v218, v170\n"
    "v_mul_f32 v219, v219, v171\n"
    "v_mul_f32 v220, v220, v172\n"
    "v_mul_f32 v221, v221, v173\n"
    "v_exp_f32 v218, v218\n"
    "v_exp_f32 v219, v219\n"
    "v_exp_f32 v220, v220\n"
    "v_exp_f32 v221, v221\n"
    "v_mul_f32 v222, v170, v202\n"
    "v_mul_f32 v223, v171, v203\n"
    "v_mul_f32 v224, v172, v204\n"
    "v_mul_f32 v225, v173, v205\n"
    "v_add_f32 v218, 1.0, v218\n"
    "v_add_f32 v219, 1.0, v219\n"
    "v_add_f32 v220, 1.0, v220\n"
    "v_add_f32 v221, 1.0, v221\n"
    "s_call_b64 s[50:51], LT33_%=\n"
    "v_rcp_f32 v218, v218\n"
    "v_rcp_f32 v219, v219\n"
    "v_rcp_f32 v220, v220\n"
    "v_rcp_f32 v221, v221\n"
    "v_fmac_f32 v21, v222, v218\n"
    "v_fmac_f32 v21, v223, v219\n"
    "v_fmac_f32 v21, v224, v220\n"
    "v_fmac_f32 v21, v225, v221\n"
    "v_mul_f32 v218, v174, v174\n"
    "v_mul_f32 v219, v175, v175\n"
    "v_mul_f32 v220, v176, v176\n"
    "v_mul_f32 v221, v177, v177\n"
    "v_fmaak_f32 v218, v218, v22, 0xc0135761\n"
    "v_fmaak_f32 v219, v219, v22, 0xc0135761\n"
    "v_fmaak_f32 v220, v220, v22, 0xc0135761\n"
    "v_fmaak_f32 v221, v221, v22, 0xc0135761\n"
    "v_mul_f32 v218, v218, v174\n"
    "v_mul_f32 v219, v219, v175\n"
    "v_mul_f32 v220, v220, v176\n"
    "v_mul_f32 v221, v221, v177\n"
    "v_exp_f32 v218, v218\n"
    "v_exp_f32 v219, v219\n"
    "v_exp_f32 v220, v220\n"
    "v_exp_f32 v221, v221\n"
    "s_call_b64 s[50:51], LT33_%=\n"
    "v_mul_f32 v222, v174, v206\n"
    "v_mul_f32 v223, v175, v207\n"
    "v_mul_f32 v224, v176, v208\n"
    "v_mul_f32 v225, v177, v209\n"
    "v_add_f32 v218, 1.0, v218\n"
    "v_add_f32 v219, 1.0, v219\n"
    "v_add_f32 v220, 1.0, v220\n"
    "v_add_f32 v221, 1.0, v221\n"
    "v_rcp_f32 v218, v218\n"
    "v_rcp_f32 v219, v219\n"
    "v_rcp_f32 v220, v220\n"
    "v_rcp_f32 v221, v221\n"
    "v_fmac_f32 v21, v222, v218\n"
    "v_fmac_f32 v21, v223, v219\n"
    "v_fmac_f32 v21, v224, v220\n"
    "v_fmac_f32 v21, v225, v221\n"
    "v_mul_f32 v218, v178, v178\n"
    "v_mul_f32 v219, v179, v179\n"
    "v_mul_f32 v220, v180, v180\n"
    "v_mul_f32 v221, v181, v181\n"
    "v_fmaak_f32 v218, v218, v22, 0xc0135761\n"
    "v_fmaak_f32 v219, v219, v22, 0xc0135761\n"
    "v_fmaak_f32 v220, v220, v22, 0xc0135761\n"
    "v_fmaak_f32 v221, v221, v22, 0xc0135761\n"
    "s_setprio 0\n"
    "s_call_b64 s[50:51], LT33_%=\n"
    "v_mul_f32 v218, v218, v178\n"
    "v_mul_f32 v219, v219, v179\n"
    "v_mul_f32 v220, v220, v180\n"
    "v_mul_f32 v221, v221, v181\n"
    "v_exp_f32 v218, v218\n"
    "v_exp_f32 v219, v219\n"
    "v_exp_f32 v220, v220\n"
    "v_exp_f32 v221, v221\n"
    "v_mul_f32 v222, v178, v210\n"
    "v_mul_f32 v223, v179, v211\n"
    "v_mul_f32 v224, v180, v212\n"
    "v_mul_f32 v225, v181, v213\n"
    "v_add_f32 v218, 1.0, v218\n"
    "v_add_f32 v219, 1.0, v219\n"
    "v_add_f32 v220, 1.0, v220\n"
    "v_add_f32 v221, 1.0, v221\n"
    "v_rcp_f32 v218, v218\n"
    "v_rcp_f32 v219, v219\n"
    "v_rcp_f32 v220, v220\n"
    "v_rcp_f32 v221, v221\n"
    "v_fmac_f32 v21, v222, v218\n"
    "v_fmac_f32 v21, v223, v219\n"
    "v_fmac_f32 v21, v224, v220\n"
    "v_fmac_f32 v21, v225, v221\n"
    "s_call_b64 s[50:51], LT33_%=\n"
    "v_mul_f32 v218, v182, v182\n"
    "v_mul_f32 v219, v183, v183\n"
    "v_mul_f32 v220, v184, v184\n"
    "v_mul_f32 v221, v185, v185\n"
    "v_fmaak_f32 v218, v218, v22, 0xc0135761\n"
    "v_fmaak_f32 v219, v219, v22, 0xc0135761\n"
    "v_fmaak_f32 v220, v220, v22, 0xc0135761\n"
    "v_fmaak_f32 v221, v221, v22, 0xc0135761\n"
    "v_mul_f32 v218, v218, v182\n"
    "v_mul_f32 v219, v219, v183\n"
    "v_mul_f32 v220, v220, v184\n"
    "v_mul_f32 v221, v221, v185\n"
    "v_exp_f32 v218, v218\n"
    "v_exp_f32 v219, v219\n"
    "v_exp_f32 v220, v220\n"
    "v_exp_f32 v221, v221\n"
    "v_mul_f32 v222, v182, v214\n"
    "v_mul_f32 v223, v183, v215\n"
    "v_mul_f32 v224, v184, v216\n"
    "v_mul_f32 v225, v185, v217\n"
    "v_add_f32 v218, 1.0, v218\n"
    "v_add_f32 v219, 1.0, v219\n"
    "v_add_f32 v220, 1.0, v220\n"
    "v_add_f32 v221, 1.0, v221\n"
    "s_call_b64 s[50:51], LT33_%=\n"
    "v_rcp_f32 v218, v218\n"
    "v_rcp_f32 v219, v219\n"
    "v_rcp_f32 v220, v220\n"
    "v_rcp_f32 v221, v221\n"
    "v_fmac_f32 v21, v222, v218\n"
    "v_fmac_f32 v21, v223, v219\n"
    "v_fmac_f32 v21, v224, v220\n"
    "v_fmac_f32 v21, v225, v221\n"
    "s_waitcnt lgkmcnt(1)\n"
    "v_mul_f32 v218, v186, v186\n"
    "v_mul_f32 v219, v187, v187\n"
    "v_mul_f32 v220, v188, v188\n"
    "v_mul_f32 v221, v189, v189\n"
    "v_fmaak_f32 v218, v218, v22, 0xc0135761\n"
    "v_fmaak_f32 v219, v219, v22, 0xc0135761\n"
    "v_fmaak_f32 v220, v220, v22, 0xc0135761\n"
    "v_fmaak_f32 v221, v221, v22, 0xc0135761\n"
    "v_mul_f32 v218, v218, v186\n"
    "v_mul_f32 v219, v219, v187\n"
    "v_mul_f32 v220, v220, v188\n"
    "v_mul_f32 v221, v221, v189\n"
    "v_exp_f32 v218, v218\n"
    "v_exp_f32 v219, v219\n"
    "v_exp_f32 v220, v220\n"
    "v_exp_f32 v221, v221\n"
    "s_call_b64 s[50:51], LT33_%=\n"
    "v_mul_f32 v222, v186, v38\n"
    "v_mul_f32 v223, v187, v39\n"
    "v_mul_f32 v224, v188, v40\n"
    "v_mul_f32 v225, v189, v41\n"
    "v_add_f32 v218, 1.0, v218\n"
    "v_add_f32 v219, 1.0, v219\n"
    "v_add_f32 v220, 1.0, v220\n"
    "v_add_f32 v221, 1.0, v221\n"
    "v_rcp_f32 v218, v218\n"
    "v_rcp_f32 v219, v219\n"
    "v_rcp_f32 v220, v220\n"
    "v_rcp_f32 v221, v221\n"
    "v_fmac_f32 v21, v222, v218\n"
    "v_fmac_f32 v21, v223, v219\n"
    "v_fmac_f32 v21, v224, v220\n"
    "v_fmac_f32 v21, v225, v221\n"
    "v_mul_f32 v218, v190, v190\n"
    "v_mul_f32 v219, v191, v191\n"
    "v_mul_f32 v220, v192, v192\n"
    "v_mul_f32 v221, v193, v193\n"
    "v_fmaak_f32 v218, v218, v22, 0xc0135761\n"
    "v_fmaak_f32 v219, v219, v22, 0xc0135761\n"
    "v_fmaak_f32 v220, v220, v22, 0xc0135761\n"
    "v_fmaak_f32 v221, v221, v22, 0xc0135761\n"
    "s_call_b64 s[50:51], LT33_%=\n"
    "v_mul_f32 v218, v218, v190\n"
    "v_mul_f32 v219, v219, v191\n"
    "v_mul_f32 v220, v220, v192\n"
    "v_mul_f32 v221, v221, v193\n"
    "v_exp_f32 v218, v218\n"
    "v_exp_f32 v219, v219\n"
    "v_exp_f32 v220, v220\n"
    "v_exp_f32 v221, v221\n"
    "v_mul_f32 v222, v190, v42\n"
    "v_mul_f32 v223, v191, v43\n"
    "v_mul_f32 v224, v192, v44\n"
    "v_mul_f32 v225, v193, v45\n"
    "v_add_f32 v218, 1.0, v218\n"
    "v_add_f32 v219, 1.0, v219\n"
    "v_add_f32 v220, 1.0, v220\n"
    "v_add_f32 v221, 1.0, v221\n"
    "v_rcp_f32 v218, v218\n"
    "v_rcp_f32 v219, v219\n"
    "v_rcp_f32 v220, v220\n"
    "v_rcp_f32 v221, v221\n"
    "v_fmac_f32 v21, v222, v218\n"
    "v_fmac_f32 v21, v223, v219\n"
    "v_fmac_f32 v21, v224, v220\n"
    "v_fmac_f32 v21, v225, v221\n"
    "s_call_b64 s[50:51], LT33_%=\n"
    "v_mul_f32 v218, v194, v194\n"
    "v_mul_f32 v219, v195, v195\n"
    "v_mul_f32 v220, v196, v196\n"
    "v_mul_f32 v221, v197, v197\n"
    "v_fmaak_f32 v218, v218, v22, 0xc0135761\n"
    "v_fmaak_f32 v219, v219, v22, 0xc0135761\n"
    "v_fmaak_f32 v220, v220, v22, 0xc0135761\n"
    "v_fmaak_f32 v221, v221, v22, 0xc0135761\n"
    "v_mul_f32 v218, v218, v194\n"
    "v_mul_f32 v219, v219, v195\n"
    "v_mul_f32 v220, v220, v196\n"
    "v_mul_f32 v221, v221, v197\n"
    "v_exp_f32 v218, v218\n"
    "v_exp_f32 v219, v219\n"
    "v_exp_f32 v220, v220\n"
    "v_exp_f32 v221, v221\n"
    "v_mul_f32 v222, v194, v46\n"
    "v_mul_f32 v223, v195, v47\n"
    "v_mul_f32 v224, v196, v48\n"
    "v_mul_f32 v225, v197, v49\n"
    "v_add_f32 v218, 1.0, v218\n"
    "v_add_f32 v219, 1.0, v219\n"
    "v_add_f32 v220, 1.0, v220\n"
    "v_add_f32 v221, 1.0, v221\n"
    "s_call_b64 s[50:51], LT33_%=\n"
    "v_rcp_f32 v218, v218\n"
    "v_rcp_f32 v219, v219\n"
    "v_rcp_f32 v220, v220\n"
    "v_rcp_f32 v221, v221\n"
    "v_fmac_f32 v21, v222, v218\n"
    "v_fmac_f32 v21, v223, v219\n"
    "v_fmac_f32 v21, v224, v220\n"
    "v_fmac_f32 v21, v225, v221\n"
    "v_mul_f32 v218, v198, v198\n"
    "v_mul_f32 v219, v199, v199\n"
    "v_mul_f32 v220, v200, v200\n"
    "v_mul_f32 v221, v201, v201\n"
    "v_fmaak_f32 v218, v218, v22, 0xc0135761\n"
    "v_fmaak_f32 v219, v219, v22, 0xc0135761\n"
    "v_fmaak_f32 v220, v220, v22, 0xc0135761\n"
    "v_fmaak_f32 v221, v221, v22, 0xc0135761\n"
    "v_mul_f32 v218, v218, v198\n"
    "v_mul_f32 v219, v219, v199\n"
    "v_mul_f32 v220, v220, v200\n"
    "v_mul_f32 v221, v221, v201\n"
    "v_exp_f32 v218, v218\n"
    "v_exp_f32 v219, v219\n"
    "v_exp_f32 v220, v220\n"
    "v_exp_f32 v221, v221\n"
    "s_call_b64 s[50:51], LT33_%=\n"
    "v_mul_f32 v222, v198, v50\n"
    "v_mul_f32 v223, v199, v51\n"
    "v_mul_f32 v224, v200, v52\n"
    "v_mul_f32 v225, v201, v53\n"
    "v_add_f32 v218, 1.0, v218\n"
    "v_add_f32 v219, 1.0, v219\n"
    "v_add_f32 v220, 1.0, v220\n"
    "v_add_f32 v221, 1.0, v221\n"
    "v_rcp_f32 v218, v218\n"
    "v_rcp_f32 v219, v219\n"
    "v_rcp_f32 v220, v220\n"
    "v_rcp_f32 v221, v221\n"
    "v_fmac_f32 v21, v222, v218\n"
    "v_fmac_f32 v21, v223, v219\n"
    "v_fmac_f32 v21, v224, v220\n"
    "v_fmac_f32 v21, v225, v221\n"
    "s_setprio 3\n"
    "v_mov_b32 v230, v21\n"
    "s_nop 1\n"
    "v_permlane32_swap_b32 v230, v21\n"
    "v_add_f32 v230, v230, v21\n"
    "s_mov_b32 exec_hi, 0\n"
    "global_store_dwordx2 v229, v[230:231], s[26:27] sc1\n"
    "s_mov_b32 exec_hi, -1\n"
    "s_branch LJ9_%=\n"
    "LT33_%=:\n"
    "s_cmp_eq_u32 s59, 6\n"
    "s_cbranch_scc1 L52_%=\n"
    "ds_read_b32 v19, v17 offset:12\n"
    "s_waitcnt vmcnt(0)\n"
    "s_bitcmp1_b32 s59, 0\n"
    "s_cbranch_scc1 L53_%=\n"
    "s_sub_u32 s45, s45, 1\n"
    "s_cselect_b32 s28, 1, s28\n"
    "s_cselect_b32 s45, 0, s45\n"
    "s_cbranch_scc1 L55_%=\n"
    "v_cmp_ne_u32 vcc, s56, v7\n"
    "s_nop 0\n"
    "s_cmp_eq_u64 vcc, 0\n"
    "s_cbranch_scc0 L54_%=\n"
    "L55_%=:\n"
    "s_waitcnt lgkmcnt(0)\n"
    "v_readfirstlane_b32 s55, v19\n"
    "s_nop 0\n"
    "s_xor_b32 s55, s55, 0x48\n"
    "s_and_b32 s55, s55, s53\n"
    "s_cbranch_scc1 L52_%=\n"
    "s_add_u32 m0, s52, 0\n"
    "s_add_u32 s48, s64, m0\n"
    "s_addc_u32 s49, s65, 0\n"
    "global_load_lds_dwordx4 v2, s[48:49] offset:0 sc1\n"
    "global_load_lds_dwordx4 v2, s[48:49] offset:1024 sc1\n"
    "global_load_lds_dwordx4 v2, s[48:49] offset:2048 sc1\n"
    "global_load_lds_dwordx4 v2, s[48:49] offset:3072 sc1\n"
    "s_add_u32 m0, s52, 4096\n"
    "s_add_u32 s48, s64, m0\n"
    "s_addc_u32 s49, s65, 0\n"
    "global_load_lds_dwordx4 v2, s[48:49] offset:0 sc1\n"
    "global_load_lds_dwordx4 v2, s[48:49] offset:1024 sc1\n"
    "global_load_lds_dwordx4 v2, s[48:49] offset:2048 sc1\n"
    "global_load_lds_dwordx4 v2, s[48:49] offset:3072 sc1\n"
    "s_add_u32 m0, s52, 8192\n"
    "s_add_u32 s48, s64, m0\n"
    "s_addc_u32 s49, s65, 0\n"
    "global_load_lds_dwordx4 v2, s[48:49] offset:0 sc1\n"
    "s_cmp_eq_u32 s54, 9\n"
    "s_cbranch_scc1 L56_%=\n"
    "global_load_lds_dwordx4 v2, s[48:49] offset:1024 sc1\n"
    "global_load_lds_dwordx4 v2, s[48:49] offset:2048 sc1\n"
    "s_cmp_eq_u32 s54, 11\n"
    "s_cbranch_scc1 L56_%=\n"
    "global_load_lds_dwordx4 v2, s[48:49] offset:3072 sc1\n"
    "s_add_u32 m0, s52, 12288\n"
    "s_add_u32 s48, s64, m0\n"
    "s_addc_u32 s49, s65, 0\n"
    "global_load_lds_dwordx4 v2, s[48:49] offset:0 sc1\n"
    "global_load_lds_dwordx4 v2, s[48:49] offset:1024 sc1\n"
    "global_load_lds_dwordx4 v2, s[48:49] offset:2048 sc1\n"
    "global_load_lds_dwordx4 v2, s[48:49] offset:3072 sc1\n"
    "s_add_u32 m0, s52, 16384\n"
    "s_add_u32 s48, s64, m0\n"
    "s_addc_u32 s49, s65, 0\n"
    "global_load_lds_dwordx4 v2, s[48:49] offset:0 sc1\n"
    "global_load_lds_dwordx4 v2, s[48:49] offset:1024 sc1\n"
    "L56_%=:\n"
    "v_and_b32 v8, 1, v6\n"
    "v_cmp_ne_u32 vcc, 0, v8\n"
    "s_nop 0\n"
    "s_cmp_eq_u64 vcc, 0\n"
    "s_cselect_b32 s28, 1, s28\n"
    "v_and_b32 v8, 2, v6\n"
    "v_cmp_ne_u32 vcc, 0, v8\n"
    "s_nop 0\n"
    "s_cmp_eq_u64 vcc, 0\n"
    "s_cselect_b32 s28, 1, s28\n"
    "v_and_b32 v8, 4, v6\n"
    "v_cmp_ne_u32 vcc, 0, v8\n"
    "s_nop 0\n"
    "s_cmp_eq_u64 vcc, 0\n"
    "s_cselect_b32 s28, 1, s28\n"
    "s_add_u32 s59, s59, 1\n"
    "s_setpc_b64 s[50:51]\n"
    "L53_%=:\n"
    "s_mov_b64 exec, 1\n"
    "ds_add_u32 v9, v18\n"
    "s_mov_b64 exec, -1\n"
    "s_add_u32 s59, s59, 1\n"
    "v_mov_b32 v4, v5\n"
    "v_mov_b32 v9, v12\n"
    "v_mov_b32 v5, v10\n"
    "v_mov_b32 v12, v17\n"
    "s_mov_b32 s52, s40\n"
    "s_mov_b32 s53, s41\n"
    "s_mov_b32 s40, s42\n"
    "s_movk_i32 s41, 0xff\n"
    "s_mov_b32 s54, s43\n"
    "s_mov_b32 s43, 9\n"
    "s_cmp_eq_u32 s59, 6\n"
    "s_cbranch_scc1 L57_%=\n"
    "L54_%=:\n"
    "global_load_dwordx2 v[6:7], v4, s[26:27] sc1\n"
    "L57_%=:\n"
    "s_waitcnt lgkmcnt(0)\n"
    "L52_%=:\n"
    "s_setpc_b64 s[50:51]\n"
    "LS44_%=:\n"
    "v_mov_b32 v17, 153728\n"
    "v_mov_b32 v18, 1\n"
    "s_mov_b32 s28, 0\n"
    "s_add_u32 s64, s26, 63488\n"
    "s_addc_u32 s65, s27, 0\n"
    "s_mov_b32 s46, 0xffffffff\n"
    "s_mov_b32 s47, 0x0\n"
    "s_lshl_b32 s40, s2, 8\n"
    "s_add_u32 s40, s40, 75776\n"
    "v_lshl_add_u32 v143, v143, 3, s40\n"
    "v_min_u32 v4, 9, v136\n"
    "v_lshlrev_b32 v144, 3, v136\n"
    "v_lshlrev_b32 v147, 3, v4\n"
    "v_add_u32 v144, 75776, v144\n"
    "v_lshl_add_u32 v146, v138, 8, v144\n"
    "v_add_u32 v146, 6144, v146\n"
    "v_lshl_add_u32 v144, v137, 8, v144\n"
    "v_add_u32 v145, 4096, v144\n"
    "v_add_u32 v147, 75904, v147\n"
    "v_lshl_add_u32 v149, v138, 8, v147\n"
    "v_add_u32 v149, 6144, v149\n"
    "v_lshl_add_u32 v147, v137, 8, v147\n"
    "v_add_u32 v148, 4096, v147\n"
    "s_waitcnt lgkmcnt(0)\n"
    "s_add_u32 s56, s56, 2654435769\n"
    "v_mov_b32 v153, s56\n"
    "s_waitcnt vmcnt(14)\n"
    "v_cndmask_b32 v30, 0, v30, s[46:47]\n"
    "v_lshlrev_b32 v45, 16, v45\n"
    "v_lshlrev_b32 v24, 16, v24\n"
    "v_mul_f32 v152, v45, v24\n"
    "v_lshlrev_b32 v46, 16, v46\n"
    "v_lshlrev_b32 v25, 16, v25\n"
    "v_fmac_f32 v152, v46, v25\n"
    "v_lshlrev_b32 v47, 16, v47\n"
    "v_lshlrev_b32 v26, 16, v26\n"
    "v_fmac_f32 v152, v47, v26\n"
    "v_lshlrev_b32 v48, 16, v48\n"
    "v_lshlrev_b32 v27, 16, v27\n"
    "v_fmac_f32 v152, v48, v27\n"
    "v_lshlrev_b32 v49, 16, v49\n"
    "v_lshlrev_b32 v28, 16, v28\n"
    "v_fmac_f32 v152, v49, v28\n"
    "v_lshlrev_b32 v50, 16, v50\n"
    "v_lshlrev_b32 v29, 16, v29\n"
    "v_fmac_f32 v152, v50, v29\n"
    "v_lshlrev_b32 v51, 16, v51\n"
    "v_lshlrev_b32 v30, 16, v30\n"
    "v_fmac_f32 v152, v51, v30\n"
    "v_mov_b32 v9, v152\n"
    "s_nop 1\n"
    "v_permlane32_swap_b32 v152, v9\n"
    "v_add_f32 v152, v152, v9\n"
    "v_mov_b32 v9, v152\n"
    "s_nop 1\n"
    "v_permlane16_swap_b32 v152, v9\n"
    "v_add_f32 v152, v152, v9\n"
    "s_mov_b64 exec, 0xffff\n"
    "global_store_dwordx2 v143, v[152:153], s[26:27] sc1\n"
    "s_mov_b64 exec, -1\n"
    "v_min_u32 v4, 49, v1\n"
    "v_subrev_u32 v6, 25, v4\n"
    "v_min_u32 v6, v4, v6\n"
    "v_sub_u32 v5, v4, v6\n"
    "v_and_b32 v5, 1, v5\n"
    "v_mad_u32_u24 v167, v5, 26, s2\n"
    "v_mul_u32_u24 v167, 400, v167\n"
    "v_lshl_add_u32 v167, v6, 4, v167\n"
    "v_add_u32 v167, 56320, v167\n"
    "v_subrev_u32 v15, 23, v6\n"
    "v_lshlrev_b32 v15, 2, v15\n"
    "v_mad_u32_u24 v15, v5, 7, v15\n"
    "v_min_u32 v6, 22, v6\n"
    "v_mul_u32_u24 v7, 316, v6\n"
    "v_lshrrev_b32 v7, 10, v7\n"
    "v_mul_u32_u24 v8, 13, v7\n"
    "v_lshlrev_b32 v9, 2, v6\n"
    "v_sub_u32 v8, v9, v8\n"
    "s_mov_b32 s44, 0x1800000\n"
    "s_mov_b32 s45, 0xffff0000\n"
    "v_mov_b32 v9, 0x45800000\n"
    "v_mov_b32 v10, 0x42800000\n"
    "v_cndmask_b32 v166, v9, v10, s[44:45]\n"
    "v_add_u32 v12, v8, v7\n"
    "v_subrev_u32 v14, 13, v12\n"
    "v_min_u32 v12, v12, v14\n"
    "v_lshl_add_u32 v13, v8, 1, v5\n"
    "v_mul_u32_u24 v154, 144, v13\n"
    "v_lshl_add_u32 v154, v12, 3, v154\n"
    "v_mul_u32_u24 v158, 288, v12\n"
    "v_lshl_add_u32 v158, v13, 2, v158\n"
    "v_add_u32 v158, 89088, v158\n"
    "v_min_u32 v162, 1, v7\n"
    "v_mul_u32_u24 v162, 0x458, v162\n"
    "v_lshlrev_b32 v162, 20, v162\n"
    "v_cndmask_b32 v162, v162, 0, s[44:45]\n"
    "v_add_u32 v14, 0, v15\n"
    "v_min_u32 v14, 12, v14\n"
    "v_lshlrev_b32 v14, 3, v14\n"
    "v_add_u32 v14, 3744, v14\n"
    "v_cndmask_b32 v154, v154, v14, s[44:45]\n"
    "v_add_u32 v154, 89088, v154\n"
    "v_add_u32 v9, 1, v8\n"
    "v_subrev_u32 v10, 13, v9\n"
    "v_min_u32 v10, v9, v10\n"
    "v_sub_u32 v11, v9, v10\n"
    "v_and_b32 v11, 1, v11\n"
    "v_add_u32 v11, v11, v7\n"
    "v_add_u32 v12, v10, v11\n"
    "v_subrev_u32 v14, 13, v12\n"
    "v_min_u32 v12, v12, v14\n"
    "v_lshl_add_u32 v13, v10, 1, v5\n"
    "v_mul_u32_u24 v155, 144, v13\n"
    "v_lshl_add_u32 v155, v12, 3, v155\n"
    "v_mul_u32_u24 v159, 288, v12\n"
    "v_lshl_add_u32 v159, v13, 2, v159\n"
    "v_add_u32 v159, 89088, v159\n"
    "v_min_u32 v163, 1, v11\n"
    "v_mul_u32_u24 v163, 0x458, v163\n"
    "v_lshlrev_b32 v163, 20, v163\n"
    "v_cndmask_b32 v163, v163, 0, s[44:45]\n"
    "v_add_u32 v14, 1, v15\n"
    "v_min_u32 v14, 12, v14\n"
    "v_lshlrev_b32 v14, 3, v14\n"
    "v_add_u32 v14, 3744, v14\n"
    "v_cndmask_b32 v155, v155, v14, s[44:45]\n"
    "v_add_u32 v155, 89088, v155\n"
    "v_add_u32 v9, 2, v8\n"
    "v_subrev_u32 v10, 13, v9\n"
    "v_min_u32 v10, v9, v10\n"
    "v_sub_u32 v11, v9, v10\n"
    "v_and_b32 v11, 1, v11\n"
    "v_add_u32 v11, v11, v7\n"
    "v_add_u32 v12, v10, v11\n"
    "v_subrev_u32 v14, 13, v12\n"
    "v_min_u32 v12, v12, v14\n"
    "v_lshl_add_u32 v13, v10, 1, v5\n"
    "v_mul_u32_u24 v156, 144, v13\n"
    "v_lshl_add_u32 v156, v12, 3, v156\n"
    "v_mul_u32_u24 v160, 288, v12\n"
    "v_lshl_add_u32 v160, v13, 2, v160\n"
    "v_add_u32 v160, 89088, v160\n"
    "v_min_u32 v164, 1, v11\n"
    "v_mul_u32_u24 v164, 0x458, v164\n"
    "v_lshlrev_b32 v164, 20, v164\n"
    "v_cndmask_b32 v164, v164, 0, s[44:45]\n"
    "v_add_u32 v14, 2, v15\n"
    "v_min_u32 v14, 12, v14\n"
    "v_lshlrev_b32 v14, 3, v14\n"
    "v_add_u32 v14, 3744, v14\n"
    "v_cndmask_b32 v156, v156, v14, s[44:45]\n"
    "v_add_u32 v156, 89088, v156\n"
    "v_add_u32 v9, 3, v8\n"
    "v_subrev_u32 v10, 13, v9\n"
    "v_min_u32 v10, v9, v10\n"
    "v_sub_u32 v11, v9, v10\n"
    "v_and_b32 v11, 1, v11\n"
    "v_add_u32 v11, v11, v7\n"
    "v_add_u32 v12, v10, v11\n"
    "v_subrev_u32 v14, 13, v12\n"
    "v_min_u32 v12, v12, v14\n"
    "v_lshl_add_u32 v13, v10, 1, v5\n"
    "v_mul_u32_u24 v157, 144, v13\n"
    "v_lshl_add_u32 v157, v12, 3, v157\n"
    "v_mul_u32_u24 v161, 288, v12\n"
    "v_lshl_add_u32 v161, v13, 2, v161\n"
    "v_add_u32 v161, 89088, v161\n"
    "v_min_u32 v165, 1, v11\n"
    "v_mul_u32_u24 v165, 0x458, v165\n"
    "v_lshlrev_b32 v165, 20, v165\n"
    "v_cndmask_b32 v165, v165, 0, s[44:45]\n"
    "v_add_u32 v14, 3, v15\n"
    "v_min_u32 v14, 12, v14\n"
    "v_lshlrev_b32 v14, 3, v14\n"
    "v_add_u32 v14, 3744, v14\n"
    "v_cndmask_b32 v157, v157, v14, s[44:45]\n"
    "v_add_u32 v157, 89088, v157\n"
    "s_lshl_b32 s40, s31, 6\n"
    "s_add_u32 s40, s40, 89088\n"
    "v_mul_u32_u24 v150, 144, v136\n"
    "v_lshl_add_u32 v150, v137, 4, v150\n"
    "v_add_u32 v150, s40, v150\n"
    "s_waitcnt vmcnt(0)\n"
    "v_lshlrev_b32 v38, 16, v38\n"
    "v_lshlrev_b32 v31, 16, v31\n"
    "v_lshlrev_b32 v39, 16, v39\n"
    "v_lshlrev_b32 v32, 16, v32\n"
    "v_lshlrev_b32 v40, 16, v40\n"
    "v_lshlrev_b32 v33, 16, v33\n"
    "v_lshlrev_b32 v41, 16, v41\n"
    "v_lshlrev_b32 v34, 16, v34\n"
    "v_lshlrev_b32 v42, 16, v42\n"
    "v_lshlrev_b32 v35, 16, v35\n"
    "v_lshlrev_b32 v43, 16, v43\n"
    "v_lshlrev_b32 v36, 16, v36\n"
    "v_lshlrev_b32 v44, 16, v44\n"
    "v_lshlrev_b32 v37, 16, v37\n"
    "v_cndmask_b32 v44, 0, v44, s[46:47]\n"
    "s_mov_b32 s48, 0x4000400\n"
    "s_mov_b32 s49, 0x4000400\n"
    "s_sleep 14\n"
    "s_mov_b32 s41, 65536\n"
    "L45_%=:\n"
    "global_load_dwordx2 v[52:53], v144, s[26:27] offset:0 sc1\n"
    "global_load_dwordx2 v[54:55], v147, s[26:27] offset:0 sc1\n"
    "global_load_dwordx2 v[56:57], v144, s[26:27] offset:1024 sc1\n"
    "global_load_dwordx2 v[58:59], v147, s[26:27] offset:1024 sc1\n"
    "global_load_dwordx2 v[60:61], v144, s[26:27] offset:2048 sc1\n"
    "global_load_dwordx2 v[62:63], v147, s[26:27] offset:2048 sc1\n"
    "global_load_dwordx2 v[64:65], v144, s[26:27] offset:3072 sc1\n"
    "global_load_dwordx2 v[66:67], v147, s[26:27] offset:3072 sc1\n"
    "global_load_dwordx2 v[68:69], v145, s[26:27] offset:0 sc1\n"
    "global_load_dwordx2 v[70:71], v148, s[26:27] offset:0 sc1\n"
    "global_load_dwordx2 v[72:73], v145, s[26:27] offset:1024 sc1\n"
    "global_load_dwordx2 v[74:75], v148, s[26:27] offset:1024 sc1\n"
    "global_load_dwordx2 v[76:77], v146, s[26:27] offset:0 sc1\n"
    "global_load_dwordx2 v[78:79], v149, s[26:27] offset:0 sc1\n"
    "s_waitcnt vmcnt(12)\n"
    "v_cndmask_b32 v54, v54, v31, s[48:49]\n"
    "v_and_b32 v9, v53, v55\n"
    "v_or_b32 v10, v53, v55\n"
    "v_mfma_f32_16x16x4_f32 v[168:171], v38, v52, 0\n"
    "v_mfma_f32_16x16x4_f32 v[172:175], v38, v54, 0\n"
    "s_waitcnt vmcnt(10)\n"
    "v_cndmask_b32 v58, v58, v32, s[48:49]\n"
    "v_bitop3_b32 v9, v57, v59, v9 bitop3:0x80\n"
    "v_or3_b32 v10, v57, v59, v10\n"
    "v_mfma_f32_16x16x4_f32 v[168:171], v39, v56, v[168:171]\n"
    "v_mfma_f32_16x16x4_f32 v[172:175], v39, v58, v[172:175]\n"
    "s_waitcnt vmcnt(8)\n"
    "v_cndmask_b32 v62, v62, v33, s[48:49]\n"
    "v_bitop3_b32 v9, v61, v63, v9 bitop3:0x80\n"
    "v_or3_b32 v10, v61, v63, v10\n"
    "v_mfma_f32_16x16x4_f32 v[168:171], v40, v60, v[168:171]\n"
    "v_mfma_f32_16x16x4_f32 v[172:175], v40, v62, v[172:175]\n"
    "s_waitcnt vmcnt(6)\n"
    "v_cndmask_b32 v66, v66, v34, s[48:49]\n"
    "v_bitop3_b32 v9, v65, v67, v9 bitop3:0x80\n"
    "v_or3_b32 v10, v65, v67, v10\n"
    "v_mfma_f32_16x16x4_f32 v[168:171], v41, v64, v[168:171]\n"
    "v_mfma_f32_16x16x4_f32 v[172:175], v41, v66, v[172:175]\n"
    "s_waitcnt vmcnt(4)\n"
    "v_cndmask_b32 v70, v70, v35, s[48:49]\n"
    "v_bitop3_b32 v9, v69, v71, v9 bitop3:0x80\n"
    "v_or3_b32 v10, v69, v71, v10\n"
    "v_mfma_f32_16x16x4_f32 v[168:171], v42, v68, v[168:171]\n"
    "v_mfma_f32_16x16x4_f32 v[172:175], v42, v70, v[172:175]\n"
    "s_waitcnt vmcnt(2)\n"
    "v_cndmask_b32 v74, v74, v36, s[48:49]\n"
    "v_bitop3_b32 v9, v73, v75, v9 bitop3:0x80\n"
    "v_or3_b32 v10, v73, v75, v10\n"
    "v_mfma_f32_16x16x4_f32 v[168:171], v43, v72, v[168:171]\n"
    "v_mfma_f32_16x16x4_f32 v[172:175], v43, v74, v[172:175]\n"
    "s_waitcnt vmcnt(0)\n"
    "v_cndmask_b32 v78, v78, v37, s[48:49]\n"
    "v_bitop3_b32 v9, v77, v79, v9 bitop3:0x80\n"
    "v_or3_b32 v10, v77, v79, v10\n"
    "v_mfma_f32_16x16x4_f32 v[168:171], v44, v76, v[168:171]\n"
    "v_mfma_f32_16x16x4_f32 v[172:175], v44, v78, v[172:175]\n"
    "v_cmp_ne_u32 vcc, s56, v9\n"
    "v_cmp_ne_u32 s[38:39], s56, v10\n"
    "s_nop 0\n"
    "s_or_b64 vcc, vcc, s[38:39]\n"
    "s_cbranch_scc0 L46_%=\n"
    "s_nop 15\n"
    "s_sub_u32 s41, s41, 1\n"
    "s_cbranch_scc0 L45_%=\n"
    "s_mov_b32 s28, 1\n"
    "L46_%=:\n"
    "s_nop 10\n"
    "ds_write_b128 v150, v[168:171]\n"
    "ds_write_b128 v150, v[172:175] offset:2304\n"
    "v_mov_b32 v13, s28\n"
    "v_lshl_add_u32 v13, v13, 4, 1\n"
    "s_mov_b64 exec, 1\n"
    "ds_add_u32 v17, v13 offset:20\n"
    "s_mov_b64 exec, -1\n"
    "s_cmp_lg_u32 s31, 0\n"
    "s_cbranch_scc1 LN8_%=\n"
    "L47_%=:\n"
    "ds_read_b32 v19, v17 offset:20\n"
    "s_waitcnt lgkmcnt(0)\n"
    "v_readfirstlane_b32 s55, v19\n"
    "s_nop 0\n"
    "s_cmp_ge_u32 s55, 2\n"
    "s_cbranch_scc1 L48_%=\n"
    "s_branch L47_%=\n"
    "L48_%=:\n"
    "s_cmp_gt_u32 s55, 2\n"
    "s_cbranch_scc1 LN8_%=\n"
    "ds_read_b64 v[80:81], v154\n"
    "ds_read2_b32 v[88:89], v158 offset0:0 offset1:36\n"
    "ds_read_b64 v[82:83], v155\n"
    "ds_read2_b32 v[90:91], v159 offset0:0 offset1:36\n"
    "ds_read_b64 v[84:85], v156\n"
    "ds_read2_b32 v[92:93], v160 offset0:0 offset1:36\n"
    "ds_read_b64 v[86:87], v157\n"
    "ds_read2_b32 v[94:95], v161 offset0:0 offset1:36\n"
    "s_mov_b32 s48, 0x1400000\n"
    "s_mov_b32 s49, 0xfffe8000\n"
    "s_mov_b32 s50, 0x0\n"
    "s_mov_b32 s51, 0xfffe0000\n"
    "s_waitcnt lgkmcnt(6)\n"
    "v_mul_f32 v88, v88, v162\n"
    "v_mul_f32 v89, v89, v162\n"
    "v_fmac_f32 v88, v80, v166\n"
    "v_fmac_f32 v89, v81, v166\n"
    "v_cvt_pk_f16_f32 v108, v88, v89\n"
    "s_waitcnt lgkmcnt(4)\n"
    "v_mul_f32 v90, v90, v163\n"
    "v_mul_f32 v91, v91, v163\n"
    "v_fmac_f32 v90, v82, v166\n"
    "v_fmac_f32 v91, v83, v166\n"
    "v_cvt_pk_f16_f32 v109, v90, v91\n"
    "s_waitcnt lgkmcnt(2)\n"
    "v_mul_f32 v92, v92, v164\n"
    "v_mul_f32 v93, v93, v164\n"
    "v_fmac_f32 v92, v84, v166\n"
    "v_fmac_f32 v93, v85, v166\n"
    "v_cvt_pk_f16_f32 v110, v92, v93\n"
    "s_waitcnt lgkmcnt(0)\n"
    "v_mul_f32 v94, v94, v165\n"
    "v_mul_f32 v95, v95, v165\n"
    "v_fmac_f32 v94, v86, v166\n"
    "v_fmac_f32 v95, v87, v166\n"
    "v_cvt_pk_f16_f32 v111, v94, v95\n"
    "v_cndmask_b32 v111, v111, 0, s[48:49]\n"
    "v_cndmask_b32 v110, v110, 0, s[50:51]\n"
    "global_store_dwordx4 v167, v[108:111], s[64:65] sc1\n"
    "v_cndmask_b32 v112, v108, 0, s[44:45]\n"
    "v_cndmask_b32 v116, 0, v108, s[44:45]\n"
    "v_cndmask_b32 v113, v109, 0, s[44:45]\n"
    "v_cndmask_b32 v117, 0, v109, s[44:45]\n"
    "v_cndmask_b32 v114, v110, 0, s[44:45]\n"
    "v_cndmask_b32 v118, 0, v110, s[44:45]\n"
    "v_cndmask_b32 v115, v111, 0, s[44:45]\n"
    "v_cndmask_b32 v119, 0, v111, s[44:45]\n"
    "s_mov_b32 s40, 4\n"
    "v_or_b32 v8, v112, v113\n"
    "v_or3_b32 v8, v114, v115, v8\n"
    "v_and_b32 v8, 0x78007800, v8\n"
    "v_cmp_ne_u32 vcc, 0, v8\n"
    "s_nop 0\n"
    "s_cmp_lg_u64 vcc, 0\n"
    "s_cselect_b32 s41, 1, 0\n"
    "s_or_b32 s40, s40, s41\n"
    "v_or_b32 v8, v116, v117\n"
    "v_or3_b32 v8, v118, v119, v8\n"
    "v_and_b32 v8, 0x78007800, v8\n"
    "v_cmp_ne_u32 vcc, 0, v8\n"
    "s_nop 0\n"
    "s_cmp_lg_u64 vcc, 0\n"
    "s_cselect_b32 s41, 2, 0\n"
    "s_or_b32 s40, s40, s41\n"
    "v_mov_b32 v6, s40\n"
    "v_mov_b32 v7, s56\n"
    "s_lshl_b32 s41, s2, 7\n"
    "s_add_u32 s41, s41, 12288\n"
    "v_lshl_add_u32 v4, v1, 14, s41\n"
    "s_waitcnt vmcnt(0)\n"
    "s_mov_b64 exec, 0x3\n"
    "global_store_dwordx2 v4, v[6:7], s[26:27] sc1\n"
    "s_mov_b64 exec, -1\n"
    "s_branch LN8_%=\n"
    "LS11_%=:\n"
    "v_mov_b32 v17, 153728\n"
    "v_mov_b32 v18, 1\n"
    "s_mov_b32 s28, 0\n"
    "v_and_b32 v4, 3, v1\n"
    "v_lshlrev_b32 v4, 3, v4\n"
    "v_add_u32 v4, 56320, v4\n"
    "s_waitcnt vmcnt(11)\n"
    "v_and_b32 v5, 8, v236\n"
    "v_lshl_add_u32 v236, v236, 5, v4\n"
    "v_lshlrev_b32 v5, 1, v5\n"
    "v_xor_b32 v236, v236, v5\n"
    "v_and_b32 v5, 8, v237\n"
    "v_lshl_add_u32 v237, v237, 5, v4\n"
    "v_lshlrev_b32 v5, 1, v5\n"
    "v_xor_b32 v237, v237, v5\n"
    "v_and_b32 v5, 8, v238\n"
    "v_lshl_add_u32 v238, v238, 5, v4\n"
    "v_lshlrev_b32 v5, 1, v5\n"
    "v_xor_b32 v238, v238, v5\n"
    "v_and_b32 v5, 8, v239\n"
    "v_lshl_add_u32 v239, v239, 5, v4\n"
    "v_lshlrev_b32 v5, 1, v5\n"
    "v_xor_b32 v239, v239, v5\n"
    "v_and_b32 v5, 8, v240\n"
    "v_lshl_add_u32 v240, v240, 5, v4\n"
    "v_lshlrev_b32 v5, 1, v5\n"
    "v_xor_b32 v240, v240, v5\n"
    "v_and_b32 v5, 8, v241\n"
    "v_lshl_add_u32 v241, v241, 5, v4\n"
    "v_lshlrev_b32 v5, 1, v5\n"
    "v_xor_b32 v241, v241, v5\n"
    "v_and_b32 v5, 8, v242\n"
    "v_lshl_add_u32 v242, v242, 5, v4\n"
    "v_lshlrev_b32 v5, 1, v5\n"
    "v_xor_b32 v242, v242, v5\n"
    "s_waitcnt vmcnt(3)\n"
    "s_mov_b64 exec, 1\n"
    "ds_add_u32 v17, v18 offset:36\n"
    "s_mov_b64 exec, -1\n"
    "v_mov_b32 v17, 153728\n"
    "v_mov_b32 v18, 1\n"
    "s_mov_b32 s28, 0\n"
    "s_setprio 2\n"
    "v_and_b32 v4, 3, v1\n"
    "v_bfe_u32 v5, v1, 3, 2\n"
    "v_lshl_add_u32 v4, v5, 2, v4\n"
    "v_lshlrev_b32 v4, 2, v4\n"
    "v_lshrrev_b64 v[128:129], v4, s[46:47]\n"
    "v_and_b32 v4, 15, v128\n"
    "v_bfe_u32 v5, v1, 2, 1\n"
    "v_mad_u32_u24 v4, v5, 13, v4\n"
    "v_lshrrev_b32 v6, 5, v1\n"
    "v_mad_u32_u24 v5, v6, 26, v4\n"
    "v_mul_u32_u24 v20, 368, v5\n"
    "v_mul_u32_u24 v21, 400, v5\n"
    "v_add_u32 v21, 56320, v21\n"
    "v_mul_u32_u24 v22, 688, v5\n"
    "v_add_u32 v23, 77824, v22\n"
    "v_add_u32 v22, 19456, v22\n"
    "s_mov_b32 s60, 0xee000000\n"
    "s_mov_b32 s61, 0xee000000\n"
    "v_bfe_u32 v5, v1, 2, 1\n"
    "v_lshl_add_u32 v5, v6, 1, v5\n"
    "v_lshlrev_b32 v5, 3, v5\n"
    "v_mov_b32 v7, 0xc05060a0\n"
    "v_bfe_u32 v5, v7, v5, 8\n"
    "v_add_u32 v5, 153792, v5\n"
    "v_cndmask_b32 v20, v20, v5, s[60:61]\n"
    "v_bfe_u32 v5, v1, 2, 1\n"
    "v_lshl_add_u32 v5, v6, 1, v5\n"
    "v_lshlrev_b32 v5, 3, v5\n"
    "v_mov_b32 v7, 0x5080b070\n"
    "v_bfe_u32 v5, v7, v5, 8\n"
    "v_add_u32 v5, 153792, v5\n"
    "v_cndmask_b32 v21, v21, v5, s[60:61]\n"
    "v_bfe_u32 v5, v1, 2, 1\n"
    "v_lshl_add_u32 v5, v6, 1, v5\n"
    "v_lshlrev_b32 v5, 3, v5\n"
    "v_mov_b32 v7, 0x60b080d0\n"
    "v_bfe_u32 v5, v7, v5, 8\n"
    "v_add_u32 v5, 153792, v5\n"
    "v_cndmask_b32 v22, v22, v5, s[60:61]\n"
    "v_bfe_u32 v5, v1, 2, 1\n"
    "v_lshl_add_u32 v5, v6, 1, v5\n"
    "v_lshlrev_b32 v5, 3, v5\n"
    "v_mov_b32 v7, 0x60b080d0\n"
    "v_bfe_u32 v5, v7, v5, 8\n"
    "v_add_u32 v5, 153792, v5\n"
    "v_cndmask_b32 v23, v23, v5, s[60:61]\n"
    "v_add_u32 v5, 153792, v2\n"
    "v_mov_b32 v132, 0\n"
    "v_mov_b32 v133, 0\n"
    "v_mov_b32 v134, 0\n"
    "v_mov_b32 v135, 0\n"
    "ds_write_b128 v5, v[132:135]\n"
    "v_lshlrev_b32 v24, 6, v6\n"
    "v_add_u32 v24, 152576, v24\n"
    "v_mul_u32_u24 v25, 0x020202, v6\n"
    "v_lshl_add_u32 v25, v6, 25, v25\n"
    "v_add_u32 v25, 0x05040100, v25\n"
    "s_mul_i32 s40, s30, 10240\n"
    "s_min_u32 s40, s40, 28672\n"
    "v_add_u32 v66, s40, v2\n"
    "v_add_u32 v67, 4096, v66\n"
    "v_add_u32 v68, 8192, v66\n"
    "v_add_u32 v69, 9216, v66\n"
    "v_min_u32 v69, 38256, v69\n"
    "v_lshrrev_b32 v70, 1, v66\n"
    "v_lshrrev_b32 v71, 1, v69\n"
    "s_add_u32 s50, s26, 36864\n"
    "s_addc_u32 s51, s27, 0\n"
    "L12_%=:\n"
    "ds_read_b32 v19, v17 offset:36\n"
    "s_waitcnt lgkmcnt(0)\n"
    "v_readfirstlane_b32 s55, v19\n"
    "s_nop 0\n"
    "s_cmp_ge_u32 s55, 4\n"
    "s_cbranch_scc1 L13_%=\n"
    "s_branch L12_%=\n"
    "L13_%=:\n"
    "s_add_u32 s56, s56, 2654435769\n"
    "ds_read_b64_tr_b16 v[156:157], v236\n"
    "ds_read_b64_tr_b16 v[158:159], v237\n"
    "ds_read_b64_tr_b16 v[160:161], v238\n"
    "ds_read_b64_tr_b16 v[162:163], v239\n"
    "ds_read_b64_tr_b16 v[164:165], v240\n"
    "ds_read_b64_tr_b16 v[166:167], v241\n"
    "ds_read_b64_tr_b16 v[168:169], v242\n"
    "v_mov_b32 v4, 0x2e800000\n"
    "s_waitcnt lgkmcnt(6)\n"
    "v_lshlrev_b32 v172, 16, v156\n"
    "v_and_b32 v179, 0xffff0000, v156\n"
    "v_max3_f32 v4, |v172|, |v179|, v4\n"
    "v_lshlrev_b32 v186, 16, v157\n"
    "s_waitcnt lgkmcnt(5)\n"
    "v_lshlrev_b32 v173, 16, v158\n"
    "v_and_b32 v180, 0xffff0000, v158\n"
    "v_max3_f32 v4, |v173|, |v180|, v4\n"
    "v_lshlrev_b32 v187, 16, v159\n"
    "v_max3_f32 v4, |v186|, |v187|, v4\n"
    "s_waitcnt lgkmcnt(4)\n"
    "v_lshlrev_b32 v174, 16, v160\n"
    "v_and_b32 v181, 0xffff0000, v160\n"
    "v_max3_f32 v4, |v174|, |v181|, v4\n"
    "v_lshlrev_b32 v188, 16, v161\n"
    "v_and_b32 v193, 0xffff0000, v161\n"
    "v_max3_f32 v4, |v188|, |v193|, v4\n"
    "s_waitcnt lgkmcnt(3)\n"
    "v_lshlrev_b32 v175, 16, v162\n"
    "v_and_b32 v182, 0xffff0000, v162\n"
    "v_max3_f32 v4, |v175|, |v182|, v4\n"
    "v_lshlrev_b32 v189, 16, v163\n"
    "v_and_b32 v194, 0xffff0000, v163\n"
    "v_max3_f32 v4, |v189|, |v194|, v4\n"
    "s_waitcnt lgkmcnt(2)\n"
    "v_lshlrev_b32 v176, 16, v164\n"
    "v_and_b32 v183, 0xffff0000, v164\n"
    "v_max3_f32 v4, |v176|, |v183|, v4\n"
    "v_lshlrev_b32 v190, 16, v165\n"
    "v_and_b32 v195, 0xffff0000, v165\n"
    "v_max3_f32 v4, |v190|, |v195|, v4\n"
    "s_waitcnt lgkmcnt(1)\n"
    "v_lshlrev_b32 v177, 16, v166\n"
    "v_and_b32 v184, 0xffff0000, v166\n"
    "v_max3_f32 v4, |v177|, |v184|, v4\n"
    "v_lshlrev_b32 v191, 16, v167\n"
    "v_and_b32 v196, 0xffff0000, v167\n"
    "v_max3_f32 v4, |v191|, |v196|, v4\n"
    "s_waitcnt lgkmcnt(0)\n"
    "v_lshlrev_b32 v178, 16, v168\n"
    "v_and_b32 v185, 0xffff0000, v168\n"
    "v_max3_f32 v4, |v178|, |v185|, v4\n"
    "v_lshlrev_b32 v192, 16, v169\n"
    "v_and_b32 v197, 0xffff0000, v169\n"
    "v_max3_f32 v4, |v192|, |v197|, v4\n"
    "v_and_b32 v4, 0x7f800000, v4\n"
    "v_sub_u32 v5, 0x82000000, v4\n"
    "v_subrev_u32 v80, 50331648, v4\n"
    "s_mov_b64 exec, 1\n"
    "ds_add_u32 v17, v18 offset:12\n"
    "s_mov_b64 exec, -1\n"
    "v_mul_f32 v172, v172, v5\n"
    "v_mul_f32 v173, v173, v5\n"
    "v_mul_f32 v174, v174, v5\n"
    "v_mul_f32 v175, v175, v5\n"
    "v_mul_f32 v176, v176, v5\n"
    "v_mul_f32 v177, v177, v5\n"
    "v_mul_f32 v178, v178, v5\n"
    "v_mul_f32 v179, v179, v5\n"
    "v_mul_f32 v180, v180, v5\n"
    "v_mul_f32 v181, v181, v5\n"
    "v_mul_f32 v182, v182, v5\n"
    "v_mul_f32 v183, v183, v5\n"
    "v_mul_f32 v184, v184, v5\n"
    "v_mul_f32 v185, v185, v5\n"
    "v_mul_f32 v186, v186, v5\n"
    "v_mul_f32 v187, v187, v5\n"
    "v_mul_f32 v188, v188, v5\n"
    "v_mul_f32 v189, v189, v5\n"
    "v_mul_f32 v190, v190, v5\n"
    "v_mul_f32 v191, v191, v5\n"
    "v_mul_f32 v192, v192, v5\n"
    "v_mul_f32 v193, v193, v5\n"
    "v_mul_f32 v194, v194, v5\n"
    "v_mul_f32 v195, v195, v5\n"
    "v_mul_f32 v196, v196, v5\n"
    "v_mul_f32 v197, v197, v5\n"
    "v_sub_u32 v87, 0x85000000, v4\n"
    "v_sub_u32 v88, 0x85000000, v4\n"
    "v_lshlrev_b32 v8, 2, v1\n"
    "v_add_u32 v8, 152576, v8\n"
    "s_waitcnt vmcnt(0)\n"
    "v_lshlrev_b32 v124, 16, v124\n"
    "v_lshlrev_b32 v125, 16, v125\n"
    "ds_write2st64_b32 v8, v124, v125 offset0:0 offset1:1\n"
    "v_cvt_pk_f16_f32 v26, v172, v173\n"
    "v_cvt_pk_f16_f32 v39, v172, v173\n"
    "v_cvt_pk_f16_f32 v27, v174, v175\n"
    "v_cvt_pk_f16_f32 v40, v174, v175\n"
    "v_cvt_pk_f16_f32 v28, v176, v177\n"
    "v_cvt_pk_f16_f32 v41, v176, v177\n"
    "v_cvt_pk_f16_f32 v29, v178, v179\n"
    "v_cvt_pk_f16_f32 v42, v178, v179\n"
    "v_cvt_pk_f16_f32 v30, v180, v181\n"
    "v_cvt_pk_f16_f32 v43, v180, v181\n"
    "v_cvt_pk_f16_f32 v31, v182, v183\n"
    "v_cvt_pk_f16_f32 v44, v182, v183\n"
    "v_cvt_pk_f16_f32 v32, v184, v185\n"
    "v_cvt_pk_f16_f32 v45, v184, v185\n"
    "v_cvt_pk_f16_f32 v33, v186, v187\n"
    "v_cvt_pk_f16_f32 v46, v186, v187\n"
    "v_cvt_pk_f16_f32 v34, v188, v189\n"
    "v_cvt_pk_f16_f32 v47, v188, v189\n"
    "v_cvt_pk_f16_f32 v35, v190, v191\n"
    "v_cvt_pk_f16_f32 v48, v190, v191\n"
    "v_cvt_pk_f16_f32 v36, v192, v193\n"
    "v_cvt_pk_f16_f32 v49, v192, v193\n"
    "v_cvt_pk_f16_f32 v37, v194, v195\n"
    "v_cvt_pk_f16_f32 v50, v194, v195\n"
    "v_cvt_pk_f16_f32 v38, v196, v197\n"
    "v_cvt_pk_f16_f32 v51, v196, v197\n"
    "v_mul_f32 v81, v80, v80\n"
    "v_permlane32_swap_b32 v87, v88\n"
    "v_permlane32_swap_b32 v26, v39\n"
    "v_permlane32_swap_b32 v27, v40\n"
    "v_permlane32_swap_b32 v28, v41\n"
    "v_permlane32_swap_b32 v29, v42\n"
    "v_permlane32_swap_b32 v30, v43\n"
    "v_permlane32_swap_b32 v31, v44\n"
    "v_permlane32_swap_b32 v32, v45\n"
    "v_permlane32_swap_b32 v33, v46\n"
    "v_permlane32_swap_b32 v34, v47\n"
    "v_permlane32_swap_b32 v35, v48\n"
    "v_permlane32_swap_b32 v36, v49\n"
    "v_permlane32_swap_b32 v37, v50\n"
    "v_permlane32_swap_b32 v38, v51\n"
    "v_perm_b32 v52, v27, v26, v25\n"
    "v_perm_b32 v59, v40, v39, v25\n"
    "v_perm_b32 v53, v29, v28, v25\n"
    "v_perm_b32 v60, v42, v41, v25\n"
    "v_perm_b32 v54, v31, v30, v25\n"
    "v_perm_b32 v61, v44, v43, v25\n"
    "v_perm_b32 v55, v33, v32, v25\n"
    "v_perm_b32 v62, v46, v45, v25\n"
    "v_perm_b32 v56, v35, v34, v25\n"
    "v_perm_b32 v63, v48, v47, v25\n"
    "v_perm_b32 v57, v37, v36, v25\n"
    "v_perm_b32 v64, v50, v49, v25\n"
    "v_perm_b32 v58, v38, v38, v25\n"
    "v_perm_b32 v65, v51, v51, v25\n"
    "ds_read_b128 v[236:239], v24 offset:384\n"
    "ds_read_b128 v[240:243], v24 offset:400\n"
    "ds_read_b128 v[244:247], v24 offset:416\n"
    "ds_read_b128 v[248:251], v24 offset:432\n"
    "v_pk_mul_f16 v92, v52, v26 op_sel_hi:[0,1]\n"
    "v_pk_mul_f16 v93, v52, v27 op_sel:[1,0]\n"
    "v_pk_mul_f16 v94, v53, v28 op_sel_hi:[0,1]\n"
    "v_pk_mul_f16 v95, v53, v29 op_sel:[1,0]\n"
    "v_pk_mul_f16 v96, v59, v39 op_sel_hi:[0,1]\n"
    "v_pk_mul_f16 v97, v59, v40 op_sel:[1,0]\n"
    "v_pk_mul_f16 v98, v60, v41 op_sel_hi:[0,1]\n"
    "v_pk_mul_f16 v99, v60, v42 op_sel:[1,0]\n"
    "global_load_dwordx4 v[140:143], v66, s[50:51] offset:0 sc1\n"
    "global_load_dwordx4 v[144:147], v66, s[50:51] offset:1024 sc1\n"
    "global_load_dwordx4 v[148:151], v66, s[50:51] offset:2048 sc1\n"
    "global_load_dwordx4 v[152:155], v66, s[50:51] offset:3072 sc1\n"
    "global_load_dwordx4 v[156:159], v67, s[50:51] offset:0 sc1\n"
    "global_load_dwordx4 v[160:163], v67, s[50:51] offset:1024 sc1\n"
    "global_load_dwordx4 v[164:167], v67, s[50:51] offset:2048 sc1\n"
    "global_load_dwordx4 v[168:171], v67, s[50:51] offset:3072 sc1\n"
    "global_load_dwordx4 v[172:175], v68, s[50:51] offset:0 sc1\n"
    "global_load_dwordx4 v[176:179], v69, s[50:51] offset:0 sc1\n"
    "s_waitcnt lgkmcnt(0)\n"
    "v_mul_f32 v204, v236, v87\n"
    "v_mul_f32 v220, v236, v88\n"
    "v_mul_f32 v205, v237, v87\n"
    "v_mul_f32 v221, v237, v88\n"
    "v_mul_f32 v206, v238, v87\n"
    "v_mul_f32 v222, v238, v88\n"
    "v_mul_f32 v207, v239, v87\n"
    "v_mul_f32 v223, v239, v88\n"
    "v_mul_f32 v208, v240, v87\n"
    "v_mul_f32 v224, v240, v88\n"
    "v_mul_f32 v209, v241, v87\n"
    "v_mul_f32 v225, v241, v88\n"
    "v_mul_f32 v210, v242, v87\n"
    "v_mul_f32 v226, v242, v88\n"
    "v_mul_f32 v211, v243, v87\n"
    "v_mul_f32 v227, v243, v88\n"
    "v_mul_f32 v212, v244, v87\n"
    "v_mul_f32 v228, v244, v88\n"
    "v_mul_f32 v213, v245, v87\n"
    "v_mul_f32 v229, v245, v88\n"
    "v_mul_f32 v214, v246, v87\n"
    "v_mul_f32 v230, v246, v88\n"
    "v_mul_f32 v215, v247, v87\n"
    "v_mul_f32 v231, v247, v88\n"
    "v_mul_f32 v216, v248, v87\n"
    "v_mul_f32 v232, v248, v88\n"
    "ds_read_b128 v[236:239], v24 offset:0\n"
    "ds_read_b128 v[240:243], v24 offset:16\n"
    "ds_read_b128 v[244:247], v24 offset:32\n"
    "ds_read_b128 v[248:251], v24 offset:48\n"
    "s_mov_b32 s38, 0\n"
    "s_mov_b32 s39, -1\n"
    "v_cndmask_b32 v108, v26, v33, s[38:39]\n"
    "v_cndmask_b32 v109, v27, v34, s[38:39]\n"
    "v_cndmask_b32 v110, v28, v35, s[38:39]\n"
    "v_cndmask_b32 v111, v29, v36, s[38:39]\n"
    "v_cndmask_b32 v112, v30, v37, s[38:39]\n"
    "v_cndmask_b32 v113, v31, v38, s[38:39]\n"
    "v_mov_b32 v114, v32\n"
    "v_mov_b32 v115, 0\n"
    "v_cndmask_b32 v116, v39, v46, s[38:39]\n"
    "v_cndmask_b32 v117, v40, v47, s[38:39]\n"
    "v_cndmask_b32 v118, v41, v48, s[38:39]\n"
    "v_cndmask_b32 v119, v42, v49, s[38:39]\n"
    "v_cndmask_b32 v120, v43, v50, s[38:39]\n"
    "v_cndmask_b32 v121, v44, v51, s[38:39]\n"
    "v_mov_b32 v122, v45\n"
    "v_mov_b32 v123, 0\n"
    "v_mul_f32 v84, v81, v80\n"
    "v_lshrrev_b32 v3, 4, v0\n"
    "v_lshlrev_b32 v16, 2, v3\n"
    "v_add_u32 v16, 153600, v16\n"
    "s_lshl_b32 s36, s2, 5\n"
    "v_lshl_add_u32 v3, v3, 1, s36\n"
    "s_mov_b32 s62, 0x10001\n"
    "s_mov_b32 s63, 0x10001\n"
    "s_movk_i32 s59, 0x207\n"
    "s_lshl_b32 s41, s30, 2\n"
    "s_add_u32 s41, s41, 153664\n"
    "v_mov_b32 v2, s41\n"
    "v_and_b32 v15, 7, v1\n"
    "v_lshlrev_b32 v15, 2, v15\n"
    "v_add_u32 v15, 153664, v15\n"
    "v_mov_b32 v6, 0x04800000\n"
    "s_mov_b64 exec, 0\n"
    "s_cmp_eq_u32 s30, 0\n"
    "s_cbranch_scc1 LM0_%=\n"
    "s_cmp_eq_u32 s30, 1\n"
    "s_cbranch_scc1 LM1_%=\n"
    "s_cmp_eq_u32 s30, 2\n"
    "s_cbranch_scc1 LM2_%=\n"
    "s_branch LM3_%=\n"
    "LMR_%=:\n"
    "s_mov_b64 exec, -1\n"
    "s_mov_b32 s45, 65536\n"
    "L14_%=:\n"
    "s_waitcnt vmcnt(9)\n"
    "ds_write2_b32 v70, v140, v142 offset0:0 offset1:1\n"
    "v_and_b32 v72, v141, v143\n"
    "v_or_b32 v73, v141, v143\n"
    "s_waitcnt vmcnt(8)\n"
    "ds_write2_b32 v70, v144, v146 offset0:128 offset1:129\n"
    "v_bitop3_b32 v72, v145, v147, v72 bitop3:0x80\n"
    "v_or3_b32 v73, v145, v147, v73\n"
    "s_waitcnt vmcnt(7)\n"
    "v_add_u32 v9, 1024, v70\n"
    "ds_write2_b32 v9, v148, v150 offset0:0 offset1:1\n"
    "v_bitop3_b32 v72, v149, v151, v72 bitop3:0x80\n"
    "v_or3_b32 v73, v149, v151, v73\n"
    "s_waitcnt vmcnt(6)\n"
    "ds_write2_b32 v9, v152, v154 offset0:128 offset1:129\n"
    "v_bitop3_b32 v72, v153, v155, v72 bitop3:0x80\n"
    "v_or3_b32 v73, v153, v155, v73\n"
    "s_waitcnt vmcnt(5)\n"
    "v_add_u32 v9, 2048, v70\n"
    "ds_write2_b32 v9, v156, v158 offset0:0 offset1:1\n"
    "v_bitop3_b32 v72, v157, v159, v72 bitop3:0x80\n"
    "v_or3_b32 v73, v157, v159, v73\n"
    "s_waitcnt vmcnt(4)\n"
    "ds_write2_b32 v9, v160, v162 offset0:128 offset1:129\n"
    "v_bitop3_b32 v72, v161, v163, v72 bitop3:0x80\n"
    "v_or3_b32 v73, v161, v163, v73\n"
    "s_waitcnt vmcnt(3)\n"
    "v_add_u32 v9, 3072, v70\n"
    "ds_write2_b32 v9, v164, v166 offset0:0 offset1:1\n"
    "v_bitop3_b32 v72, v165, v167, v72 bitop3:0x80\n"
    "v_or3_b32 v73, v165, v167, v73\n"
    "s_waitcnt vmcnt(2)\n"
    "ds_write2_b32 v9, v168, v170 offset0:128 offset1:129\n"
    "v_bitop3_b32 v72, v169, v171, v72 bitop3:0x80\n"
    "v_or3_b32 v73, v169, v171, v73\n"
    "s_waitcnt vmcnt(1)\n"
    "v_add_u32 v9, 4096, v70\n"
    "ds_write2_b32 v9, v172, v174 offset0:0 offset1:1\n"
    "v_bitop3_b32 v72, v173, v175, v72 bitop3:0x80\n"
    "v_or3_b32 v73, v173, v175, v73\n"
    "s_waitcnt vmcnt(0)\n"
    "ds_write2_b32 v71, v176, v178 offset0:0 offset1:1\n"
    "v_bitop3_b32 v72, v177, v179, v72 bitop3:0x80\n"
    "v_or3_b32 v73, v177, v179, v73\n"
    "v_cmp_ne_u32 vcc, s56, v72\n"
    "v_cmp_ne_u32 s[38:39], s56, v73\n"
    "s_nop 0\n"
    "s_or_b64 vcc, vcc, s[38:39]\n"
    "s_cbranch_scc0 L15_%=\n"
    "s_sub_u32 s45, s45, 1\n"
    "s_cselect_b32 s28, 1, s28\n"
    "s_cbranch_scc1 L15_%=\n"
    "global_load_dwordx4 v[140:143], v66, s[50:51] offset:0 sc1\n"
    "global_load_dwordx4 v[144:147], v66, s[50:51] offset:1024 sc1\n"
    "global_load_dwordx4 v[148:151], v66, s[50:51] offset:2048 sc1\n"
    "global_load_dwordx4 v[152:155], v66, s[50:51] offset:3072 sc1\n"
    "global_load_dwordx4 v[156:159], v67, s[50:51] offset:0 sc1\n"
    "global_load_dwordx4 v[160:163], v67, s[50:51] offset:1024 sc1\n"
    "global_load_dwordx4 v[164:167], v67, s[50:51] offset:2048 sc1\n"
    "global_load_dwordx4 v[168:171], v67, s[50:51] offset:3072 sc1\n"
    "global_load_dwordx4 v[172:175], v68, s[50:51] offset:0 sc1\n"
    "global_load_dwordx4 v[176:179], v69, s[50:51] offset:0 sc1\n"
    "s_branch L14_%=\n"
    "L15_%=:\n"
    "s_mov_b64 exec, 1\n"
    "ds_add_u32 v17, v18 offset:4\n"
    "s_mov_b64 exec, -1\n"
    "L16_%=:\n"
    "ds_read_b32 v19, v17 offset:4\n"
    "s_waitcnt lgkmcnt(0)\n"
    "v_readfirstlane_b32 s55, v19\n"
    "s_nop 0\n"
    "s_cmp_ge_u32 s55, 4\n"
    "s_cbranch_scc1 L17_%=\n"
    "s_branch L16_%=\n"
    "L17_%=:\n"
    "ds_read_b128 v[124:127], v20 offset:0\n"
    "ds_read_b128 v[128:131], v20 offset:16\n"
    "ds_read_b128 v[132:135], v20 offset:32\n"
    "s_waitcnt lgkmcnt(2)\n"
    "v_mfma_f32_32x32x16_f16 v[140:155], v[124:127], v[92:95], 0\n"
    "ds_read_b128 v[136:139], v20 offset:48\n"
    "v_pk_mul_f16 v100, v54, v30 op_sel_hi:[0,1]\n"
    "v_pk_mul_f16 v101, v54, v31 op_sel:[1,0]\n"
    "v_pk_mul_f16 v102, v55, v32 op_sel_hi:[0,1]\n"
    "v_pk_mul_f16 v103, v55, v33 op_sel:[1,0]\n"
    "v_mfma_f32_32x32x16_f16 v[156:171], v[124:127], v[96:99], 0\n"
    "v_pk_mul_f16 v104, v61, v43 op_sel_hi:[0,1]\n"
    "v_pk_mul_f16 v105, v61, v44 op_sel:[1,0]\n"
    "v_pk_mul_f16 v106, v62, v45 op_sel_hi:[0,1]\n"
    "v_pk_mul_f16 v107, v62, v46 op_sel:[1,0]\n"
    "s_waitcnt lgkmcnt(1)\n"
    "v_mfma_f32_32x32x16_f16 v[140:155], v[128:131], v[100:103], v[140:155]\n"
    "ds_read_b128 v[124:127], v20 offset:64\n"
    "v_pk_mul_f16 v92, v56, v34 op_sel_hi:[0,1]\n"
    "v_pk_mul_f16 v93, v56, v35 op_sel:[1,0]\n"
    "v_pk_mul_f16 v94, v57, v36 op_sel_hi:[0,1]\n"
    "v_pk_mul_f16 v95, v57, v37 op_sel:[1,0]\n"
    "v_mfma_f32_32x32x16_f16 v[156:171], v[128:131], v[104:107], v[156:171]\n"
    "v_pk_mul_f16 v96, v63, v47 op_sel_hi:[0,1]\n"
    "v_pk_mul_f16 v97, v63, v48 op_sel:[1,0]\n"
    "v_pk_mul_f16 v98, v64, v49 op_sel_hi:[0,1]\n"
    "v_pk_mul_f16 v99, v64, v50 op_sel:[1,0]\n"
    "v_mfma_f32_32x32x16_f16 v[140:155], v[132:135], v[92:95], v[140:155]\n"
    "ds_read_b128 v[128:131], v20 offset:80\n"
    "v_pk_mul_f16 v100, v58, v38 op_sel_hi:[0,1]\n"
    "v_pk_mul_f16 v101, v52, v27 op_sel_hi:[0,1]\n"
    "v_pk_mul_f16 v102, v52, v28 op_sel:[1,0]\n"
    "v_pk_mul_f16 v103, v53, v29 op_sel_hi:[0,1]\n"
    "v_mfma_f32_32x32x16_f16 v[156:171], v[132:135], v[96:99], v[156:171]\n"
    "v_pk_mul_f16 v104, v65, v51 op_sel_hi:[0,1]\n"
    "v_pk_mul_f16 v105, v59, v40 op_sel_hi:[0,1]\n"
    "v_pk_mul_f16 v106, v59, v41 op_sel:[1,0]\n"
    "v_pk_mul_f16 v107, v60, v42 op_sel_hi:[0,1]\n"
    "s_waitcnt lgkmcnt(1)\n"
    "v_mfma_f32_32x32x16_f16 v[140:155], v[136:139], v[100:103], v[140:155]\n"
    "ds_read_b128 v[132:135], v20 offset:96\n"
    "v_pk_mul_f16 v92, v53, v30 op_sel:[1,0]\n"
    "v_pk_mul_f16 v93, v54, v31 op_sel_hi:[0,1]\n"
    "v_pk_mul_f16 v94, v54, v32 op_sel:[1,0]\n"
    "v_pk_mul_f16 v95, v55, v33 op_sel_hi:[0,1]\n"
    "v_mfma_f32_32x32x16_f16 v[156:171], v[136:139], v[104:107], v[156:171]\n"
    "v_pk_mul_f16 v96, v60, v43 op_sel:[1,0]\n"
    "v_pk_mul_f16 v97, v61, v44 op_sel_hi:[0,1]\n"
    "v_pk_mul_f16 v98, v61, v45 op_sel:[1,0]\n"
    "v_pk_mul_f16 v99, v62, v46 op_sel_hi:[0,1]\n"
    "v_mfma_f32_32x32x16_f16 v[140:155], v[124:127], v[92:95], v[140:155]\n"
    "ds_read_b128 v[136:139], v20 offset:112\n"
    "v_pk_mul_f16 v100, v55, v34 op_sel:[1,0]\n"
    "v_pk_mul_f16 v101, v56, v35 op_sel_hi:[0,1]\n"
    "v_pk_mul_f16 v102, v56, v36 op_sel:[1,0]\n"
    "v_pk_mul_f16 v103, v57, v37 op_sel_hi:[0,1]\n"
    "v_mfma_f32_32x32x16_f16 v[156:171], v[124:127], v[96:99], v[156:171]\n"
    "v_pk_mul_f16 v104, v62, v47 op_sel:[1,0]\n"
    "v_pk_mul_f16 v105, v63, v48 op_sel_hi:[0,1]\n"
    "v_pk_mul_f16 v106, v63, v49 op_sel:[1,0]\n"
    "v_pk_mul_f16 v107, v64, v50 op_sel_hi:[0,1]\n"
    "s_waitcnt lgkmcnt(1)\n"
    "v_mfma_f32_32x32x16_f16 v[140:155], v[128:131], v[100:103], v[140:155]\n"
    "ds_read_b128 v[124:127], v20 offset:128\n"
    "v_pk_mul_f16 v92, v57, v38 op_sel:[1,0]\n"
    "v_pk_mul_f16 v93, v58, v26 op_sel_hi:[0,1]\n"
    "v_pk_mul_f16 v94, v52, v28 op_sel_hi:[0,1]\n"
    "v_pk_mul_f16 v95, v52, v29 op_sel:[1,0]\n"
    "v_mfma_f32_32x32x16_f16 v[156:171], v[128:131], v[104:107], v[156:171]\n"
    "v_pk_mul_f16 v96, v64, v51 op_sel:[1,0]\n"
    "v_pk_mul_f16 v97, v65, v39 op_sel_hi:[0,1]\n"
    "v_pk_mul_f16 v98, v59, v41 op_sel_hi:[0,1]\n"
    "v_pk_mul_f16 v99, v59, v42 op_sel:[1,0]\n"
    "v_mfma_f32_32x32x16_f16 v[140:155], v[132:135], v[92:95], v[140:155]\n"
    "ds_read_b128 v[128:131], v20 offset:144\n"
    "v_pk_mul_f16 v100, v53, v30 op_sel_hi:[0,1]\n"
    "v_pk_mul_f16 v101, v53, v31 op_sel:[1,0]\n"
    "v_pk_mul_f16 v102, v54, v32 op_sel_hi:[0,1]\n"
    "v_pk_mul_f16 v103, v54, v33 op_sel:[1,0]\n"
    "v_mfma_f32_32x32x16_f16 v[156:171], v[132:135], v[96:99], v[156:171]\n"
    "v_pk_mul_f16 v104, v60, v43 op_sel_hi:[0,1]\n"
    "v_pk_mul_f16 v105, v60, v44 op_sel:[1,0]\n"
    "v_pk_mul_f16 v106, v61, v45 op_sel_hi:[0,1]\n"
    "v_pk_mul_f16 v107, v61, v46 op_sel:[1,0]\n"
    "s_waitcnt lgkmcnt(1)\n"
    "v_mfma_f32_32x32x16_f16 v[140:155], v[136:139], v[100:103], v[140:155]\n"
    "ds_read_b128 v[132:135], v20 offset:160\n"
    "v_pk_mul_f16 v92, v55, v34 op_sel_hi:[0,1]\n"
    "v_pk_mul_f16 v93, v55, v35 op_sel:[1,0]\n"
    "v_pk_mul_f16 v94, v56, v36 op_sel_hi:[0,1]\n"
    "v_pk_mul_f16 v95, v56, v37 op_sel:[1,0]\n"
    "v_mfma_f32_32x32x16_f16 v[156:171], v[136:139], v[104:107], v[156:171]\n"
    "v_pk_mul_f16 v96, v62, v47 op_sel_hi:[0,1]\n"
    "v_pk_mul_f16 v97, v62, v48 op_sel:[1,0]\n"
    "v_pk_mul_f16 v98, v63, v49 op_sel_hi:[0,1]\n"
    "v_pk_mul_f16 v99, v63, v50 op_sel:[1,0]\n"
    "v_mfma_f32_32x32x16_f16 v[140:155], v[124:127], v[92:95], v[140:155]\n"
    "ds_read_b128 v[136:139], v20 offset:176\n"
    "v_pk_mul_f16 v100, v57, v38 op_sel_hi:[0,1]\n"
    "v_pk_mul_f16 v101, v57, v26 op_sel:[1,0]\n"
    "v_pk_mul_f16 v102, v58, v27 op_sel_hi:[0,1]\n"
    "v_pk_mul_f16 v103, v52, v29 op_sel_hi:[0,1]\n"
    "v_mfma_f32_32x32x16_f16 v[156:171], v[124:127], v[96:99], v[156:171]\n"
    "v_pk_mul_f16 v104, v64, v51 op_sel_hi:[0,1]\n"
    "v_pk_mul_f16 v105, v64, v39 op_sel:[1,0]\n"
    "v_pk_mul_f16 v106, v65, v40 op_sel_hi:[0,1]\n"
    "v_pk_mul_f16 v107, v59, v42 op_sel_hi:[0,1]\n"
    "s_waitcnt lgkmcnt(1)\n"
    "v_mfma_f32_32x32x16_f16 v[140:155], v[128:131], v[100:103], v[140:155]\n"
    "ds_read_b128 v[124:127], v20 offset:192\n"
    "v_pk_mul_f16 v92, v52, v30 op_sel:[1,0]\n"
    "v_pk_mul_f16 v93, v53, v31 op_sel_hi:[0,1]\n"
    "v_pk_mul_f16 v94, v53, v32 op_sel:[1,0]\n"
    "v_pk_mul_f16 v95, v54, v33 op_sel_hi:[0,1]\n"
    "v_mfma_f32_32x32x16_f16 v[156:171], v[128:131], v[104:107], v[156:171]\n"
    "v_pk_mul_f16 v96, v59, v43 op_sel:[1,0]\n"
    "v_pk_mul_f16 v97, v60, v44 op_sel_hi:[0,1]\n"
    "v_pk_mul_f16 v98, v60, v45 op_sel:[1,0]\n"
    "v_pk_mul_f16 v99, v61, v46 op_sel_hi:[0,1]\n"
    "v_mfma_f32_32x32x16_f16 v[140:155], v[132:135], v[92:95], v[140:155]\n"
    "ds_read_b128 v[128:131], v20 offset:208\n"
    "v_pk_mul_f16 v100, v54, v34 op_sel:[1,0]\n"
    "v_pk_mul_f16 v101, v55, v35 op_sel_hi:[0,1]\n"
    "v_pk_mul_f16 v102, v55, v36 op_sel:[1,0]\n"
    "v_pk_mul_f16 v103, v56, v37 op_sel_hi:[0,1]\n"
    "v_mfma_f32_32x32x16_f16 v[156:171], v[132:135], v[96:99], v[156:171]\n"
    "v_pk_mul_f16 v104, v61, v47 op_sel:[1,0]\n"
    "v_pk_mul_f16 v105, v62, v48 op_sel_hi:[0,1]\n"
    "v_pk_mul_f16 v106, v62, v49 op_sel:[1,0]\n"
    "v_pk_mul_f16 v107, v63, v50 op_sel_hi:[0,1]\n"
    "s_waitcnt lgkmcnt(1)\n"
    "v_mfma_f32_32x32x16_f16 v[140:155], v[136:139], v[100:103], v[140:155]\n"
    "ds_read_b128 v[132:135], v20 offset:224\n"
    "v_pk_mul_f16 v92, v56, v38 op_sel:[1,0]\n"
    "v_pk_mul_f16 v93, v57, v26 op_sel_hi:[0,1]\n"
    "v_pk_mul_f16 v94, v57, v27 op_sel:[1,0]\n"
    "v_pk_mul_f16 v95, v58, v28 op_sel_hi:[0,1]\n"
    "v_mfma_f32_32x32x16_f16 v[156:171], v[136:139], v[104:107], v[156:171]\n"
    "v_pk_mul_f16 v96, v63, v51 op_sel:[1,0]\n"
    "v_pk_mul_f16 v97, v64, v39 op_sel_hi:[0,1]\n"
    "v_pk_mul_f16 v98, v64, v40 op_sel:[1,0]\n"
    "v_pk_mul_f16 v99, v65, v41 op_sel_hi:[0,1]\n"
    "v_mfma_f32_32x32x16_f16 v[140:155], v[124:127], v[92:95], v[140:155]\n"
    "ds_read_b128 v[136:139], v20 offset:240\n"
    "v_pk_mul_f16 v100, v52, v30 op_sel_hi:[0,1]\n"
    "v_pk_mul_f16 v101, v52, v31 op_sel:[1,0]\n"
    "v_pk_mul_f16 v102, v53, v32 op_sel_hi:[0,1]\n"
    "v_pk_mul_f16 v103, v53, v33 op_sel:[1,0]\n"
    "v_mfma_f32_32x32x16_f16 v[156:171], v[124:127], v[96:99], v[156:171]\n"
    "v_pk_mul_f16 v104, v59, v43 op_sel_hi:[0,1]\n"
    "v_pk_mul_f16 v105, v59, v44 op_sel:[1,0]\n"
    "v_pk_mul_f16 v106, v60, v45 op_sel_hi:[0,1]\n"
    "v_pk_mul_f16 v107, v60, v46 op_sel:[1,0]\n"
    "s_waitcnt lgkmcnt(1)\n"
    "v_mfma_f32_32x32x16_f16 v[140:155], v[128:131], v[100:103], v[140:155]\n"
    "ds_read_b128 v[124:127], v20 offset:256\n"
    "v_pk_mul_f16 v92, v54, v34 op_sel_hi:[0,1]\n"
    "v_pk_mul_f16 v93, v54, v35 op_sel:[1,0]\n"
    "v_pk_mul_f16 v94, v55, v36 op_sel_hi:[0,1]\n"
    "v_pk_mul_f16 v95, v55, v37 op_sel:[1,0]\n"
    "v_mfma_f32_32x32x16_f16 v[156:171], v[128:131], v[104:107], v[156:171]\n"
    "v_pk_mul_f16 v96, v61, v47 op_sel_hi:[0,1]\n"
    "v_pk_mul_f16 v97, v61, v48 op_sel:[1,0]\n"
    "v_pk_mul_f16 v98, v62, v49 op_sel_hi:[0,1]\n"
    "v_pk_mul_f16 v99, v62, v50 op_sel:[1,0]\n"
    "v_mfma_f32_32x32x16_f16 v[140:155], v[132:135], v[92:95], v[140:155]\n"
    "ds_read_b128 v[128:131], v20 offset:272\n"
    "v_pk_mul_f16 v100, v56, v38 op_sel_hi:[0,1]\n"
    "v_pk_mul_f16 v101, v56, v26 op_sel:[1,0]\n"
    "v_pk_mul_f16 v102, v57, v27 op_sel_hi:[0,1]\n"
    "v_pk_mul_f16 v103, v57, v28 op_sel:[1,0]\n"
    "v_mfma_f32_32x32x16_f16 v[156:171], v[132:135], v[96:99], v[156:171]\n"
    "v_pk_mul_f16 v104, v63, v51 op_sel_hi:[0,1]\n"
    "v_pk_mul_f16 v105, v63, v39 op_sel:[1,0]\n"
    "v_pk_mul_f16 v106, v64, v40 op_sel_hi:[0,1]\n"
    "v_pk_mul_f16 v107, v64, v41 op_sel:[1,0]\n"
    "s_waitcnt lgkmcnt(1)\n"
    "v_mfma_f32_32x32x16_f16 v[140:155], v[136:139], v[100:103], v[140:155]\n"
    "ds_read_b128 v[132:135], v20 offset:288\n"
    "v_pk_mul_f16 v92, v58, v29 op_sel_hi:[0,1]\n"
    "v_pk_mul_f16 v93, v52, v31 op_sel_hi:[0,1]\n"
    "v_pk_mul_f16 v94, v52, v32 op_sel:[1,0]\n"
    "v_pk_mul_f16 v95, v53, v33 op_sel_hi:[0,1]\n"
    "v_mfma_f32_32x32x16_f16 v[156:171], v[136:139], v[104:107], v[156:171]\n"
    "v_pk_mul_f16 v96, v65, v42 op_sel_hi:[0,1]\n"
    "v_pk_mul_f16 v97, v59, v44 op_sel_hi:[0,1]\n"
    "v_pk_mul_f16 v98, v59, v45 op_sel:[1,0]\n"
    "v_pk_mul_f16 v99, v60, v46 op_sel_hi:[0,1]\n"
    "v_mfma_f32_32x32x16_f16 v[140:155], v[124:127], v[92:95], v[140:155]\n"
    "ds_read_b128 v[136:139], v20 offset:304\n"
    "v_pk_mul_f16 v100, v53, v34 op_sel:[1,0]\n"
    "v_pk_mul_f16 v101, v54, v35 op_sel_hi:[0,1]\n"
    "v_pk_mul_f16 v102, v54, v36 op_sel:[1,0]\n"
    "v_pk_mul_f16 v103, v55, v37 op_sel_hi:[0,1]\n"
    "v_mfma_f32_32x32x16_f16 v[156:171], v[124:127], v[96:99], v[156:171]\n"
    "v_pk_mul_f16 v104, v60, v47 op_sel:[1,0]\n"
    "v_pk_mul_f16 v105, v61, v48 op_sel_hi:[0,1]\n"
    "v_pk_mul_f16 v106, v61, v49 op_sel:[1,0]\n"
    "v_pk_mul_f16 v107, v62, v50 op_sel_hi:[0,1]\n"
    "s_waitcnt lgkmcnt(1)\n"
    "v_mfma_f32_32x32x16_f16 v[140:155], v[128:131], v[100:103], v[140:155]\n"
    "ds_read_b128 v[124:127], v20 offset:320\n"
    "v_pk_mul_f16 v92, v55, v38 op_sel:[1,0]\n"
    "v_pk_mul_f16 v93, v56, v26 op_sel_hi:[0,1]\n"
    "v_pk_mul_f16 v94, v56, v27 op_sel:[1,0]\n"
    "v_pk_mul_f16 v95, v57, v28 op_sel_hi:[0,1]\n"
    "v_mfma_f32_32x32x16_f16 v[156:171], v[128:131], v[104:107], v[156:171]\n"
    "v_pk_mul_f16 v96, v62, v51 op_sel:[1,0]\n"
    "v_pk_mul_f16 v97, v63, v39 op_sel_hi:[0,1]\n"
    "v_pk_mul_f16 v98, v63, v40 op_sel:[1,0]\n"
    "v_pk_mul_f16 v99, v64, v41 op_sel_hi:[0,1]\n"
    "ds_read_b32 v19, v17 offset:8\n"
    "v_mfma_f32_32x32x16_f16 v[140:155], v[132:135], v[92:95], v[140:155]\n"
    "ds_read_b128 v[128:131], v20 offset:336\n"
    "v_pk_mul_f16 v100, v57, v29 op_sel:[1,0]\n"
    "v_pk_mul_f16 v101, v58, v30 op_sel_hi:[0,1]\n"
    "v_pk_mul_f16 v102, v52, v32 op_sel_hi:[0,1]\n"
    "v_pk_mul_f16 v103, v52, v33 op_sel:[1,0]\n"
    "v_mfma_f32_32x32x16_f16 v[156:171], v[132:135], v[96:99], v[156:171]\n"
    "v_pk_mul_f16 v104, v64, v42 op_sel:[1,0]\n"
    "v_pk_mul_f16 v105, v65, v43 op_sel_hi:[0,1]\n"
    "v_pk_mul_f16 v106, v59, v45 op_sel_hi:[0,1]\n"
    "v_pk_mul_f16 v107, v59, v46 op_sel:[1,0]\n"
    "s_waitcnt lgkmcnt(2)\n"
    "v_mfma_f32_32x32x16_f16 v[140:155], v[136:139], v[100:103], v[140:155]\n"
    "ds_read_b128 v[132:135], v20 offset:352\n"
    "v_pk_mul_f16 v92, v53, v34 op_sel_hi:[0,1]\n"
    "v_pk_mul_f16 v93, v53, v35 op_sel:[1,0]\n"
    "v_pk_mul_f16 v94, v54, v36 op_sel_hi:[0,1]\n"
    "v_pk_mul_f16 v95, v54, v37 op_sel:[1,0]\n"
    "v_mfma_f32_32x32x16_f16 v[156:171], v[136:139], v[104:107], v[156:171]\n"
    "v_pk_mul_f16 v96, v60, v47 op_sel_hi:[0,1]\n"
    "v_pk_mul_f16 v97, v60, v48 op_sel:[1,0]\n"
    "v_pk_mul_f16 v98, v61, v49 op_sel_hi:[0,1]\n"
    "v_pk_mul_f16 v99, v61, v50 op_sel:[1,0]\n"
    "s_waitcnt lgkmcnt(2)\n"
    "v_cmp_gt_u32 vcc, 4, v19\n"
    "s_nop 0\n"
    "s_cbranch_vccnz L19_%=\n"
    "L18_%=:\n"
    "v_mfma_f32_32x32x16_f16 v[140:155], v[124:127], v[92:95], v[140:155]\n"
    "ds_read_b128 v[136:139], v22 offset:0\n"
    "v_pk_mul_f16 v100, v55, v38 op_sel_hi:[0,1]\n"
    "v_pk_mul_f16 v101, v55, v26 op_sel:[1,0]\n"
    "v_pk_mul_f16 v102, v56, v27 op_sel_hi:[0,1]\n"
    "v_pk_mul_f16 v103, v56, v28 op_sel:[1,0]\n"
    "v_mfma_f32_32x32x16_f16 v[156:171], v[124:127], v[96:99], v[156:171]\n"
    "v_pk_mul_f16 v104, v62, v51 op_sel_hi:[0,1]\n"
    "v_pk_mul_f16 v105, v62, v39 op_sel:[1,0]\n"
    "v_pk_mul_f16 v106, v63, v40 op_sel_hi:[0,1]\n"
    "v_pk_mul_f16 v107, v63, v41 op_sel:[1,0]\n"
    "s_waitcnt lgkmcnt(1)\n"
    "v_mfma_f32_32x32x16_f16 v[140:155], v[128:131], v[100:103], v[140:155]\n"
    "ds_read_b128 v[124:127], v22 offset:16\n"
    "v_pk_mul_f16 v92, v57, v29 op_sel_hi:[0,1]\n"
    "v_pk_mul_f16 v93, v57, v30 op_sel:[1,0]\n"
    "v_pk_mul_f16 v94, v58, v31 op_sel_hi:[0,1]\n"
    "v_mfma_f32_32x32x16_f16 v[156:171], v[128:131], v[104:107], v[156:171]\n"
    "v_pk_mul_f16 v96, v64, v42 op_sel_hi:[0,1]\n"
    "v_pk_mul_f16 v97, v64, v43 op_sel:[1,0]\n"
    "v_pk_mul_f16 v98, v65, v44 op_sel_hi:[0,1]\n"
    "v_mfma_f32_32x32x16_f16 v[140:155], v[132:135], v[92:95], v[140:155]\n"
    "ds_read_b128 v[128:131], v22 offset:32\n"
    "v_mfma_f32_32x32x16_f16 v[156:171], v[132:135], v[96:99], v[156:171]\n"
    "s_nop 9\n"
    "v_max3_f32 v8, |v140|, |v141|, |v142|\n"
    "v_max3_f32 v9, |v143|, |v144|, |v145|\n"
    "v_max3_f32 v10, |v146|, |v147|, |v148|\n"
    "v_max3_f32 v11, |v149|, |v150|, |v151|\n"
    "v_max3_f32 v8, v8, v9, |v152|\n"
    "v_max3_f32 v4, v10, v11, v8\n"
    "v_max3_f32 v8, |v156|, |v157|, |v158|\n"
    "v_max3_f32 v9, |v159|, |v160|, |v161|\n"
    "v_max3_f32 v10, |v162|, |v163|, |v164|\n"
    "v_max3_f32 v11, |v165|, |v166|, |v167|\n"
    "v_max3_f32 v8, v8, v9, |v168|\n"
    "v_max3_f32 v5, v10, v11, v8\n"
    "s_nop 1\n"
    "v_permlane32_swap_b32 v4, v5\n"
    "v_max3_f32 v4, v4, v5, v6\n"
    "v_and_b32 v4, 0x7f800000, v4\n"
    "v_sub_u32 v89, 0x82800000, v4\n"
    "v_sub_u32 v90, 0x82800000, v4\n"
    "v_subrev_u32 v7, 58720256, v4\n"
    "s_nop 0\n"
    "v_permlane32_swap_b32 v89, v90\n"
    "v_mul_f32 v8, v140, v89\n"
    "v_mul_f32 v9, v144, v89\n"
    "v_mul_f32 v10, v156, v90\n"
    "v_mul_f32 v11, v160, v90\n"
    "v_cvt_pk_f16_f32 v66, v8, v9\n"
    "v_cvt_pk_f16_f32 v73, v10, v11\n"
    "v_pk_mul_f16 v100, v66, v26 op_sel_hi:[0,1]\n"
    "v_pk_mul_f16 v101, v66, v27 op_sel_hi:[0,1]\n"
    "v_pk_mul_f16 v102, v66, v28 op_sel_hi:[0,1]\n"
    "v_pk_mul_f16 v103, v66, v29 op_sel_hi:[0,1]\n"
    "s_waitcnt lgkmcnt(2)\n"
    "s_nop 0\n"
    "v_mfma_f32_32x32x16_f16 v[172:187], v[136:139], v[100:103], 0\n"
    "v_pk_mul_f16 v104, v73, v39 op_sel_hi:[0,1]\n"
    "v_pk_mul_f16 v105, v73, v40 op_sel_hi:[0,1]\n"
    "v_pk_mul_f16 v106, v73, v41 op_sel_hi:[0,1]\n"
    "v_pk_mul_f16 v107, v73, v42 op_sel_hi:[0,1]\n"
    "ds_read_b128 v[132:135], v22 offset:48\n"
    "v_pk_mul_f16 v92, v66, v30 op_sel_hi:[0,1]\n"
    "v_pk_mul_f16 v93, v66, v31 op_sel_hi:[0,1]\n"
    "v_pk_mul_f16 v94, v66, v32 op_sel_hi:[0,1]\n"
    "v_pk_mul_f16 v95, v66, v33 op_sel_hi:[0,1]\n"
    "v_mfma_f32_32x32x16_f16 v[188:203], v[136:139], v[104:107], 0\n"
    "v_pk_mul_f16 v96, v73, v43 op_sel_hi:[0,1]\n"
    "v_pk_mul_f16 v97, v73, v44 op_sel_hi:[0,1]\n"
    "v_pk_mul_f16 v98, v73, v45 op_sel_hi:[0,1]\n"
    "v_pk_mul_f16 v99, v73, v46 op_sel_hi:[0,1]\n"
    "v_mul_f32 v8, v145, v89\n"
    "s_waitcnt lgkmcnt(1)\n"
    "v_mfma_f32_32x32x16_f16 v[172:187], v[124:127], v[92:95], v[172:187]\n"
    "ds_read_b128 v[136:139], v22 offset:64\n"
    "v_pk_mul_f16 v100, v66, v34 op_sel_hi:[0,1]\n"
    "v_pk_mul_f16 v101, v66, v35 op_sel_hi:[0,1]\n"
    "v_pk_mul_f16 v102, v66, v36 op_sel_hi:[0,1]\n"
    "v_pk_mul_f16 v103, v66, v37 op_sel_hi:[0,1]\n"
    "v_mfma_f32_32x32x16_f16 v[188:203], v[124:127], v[96:99], v[188:203]\n"
    "v_pk_mul_f16 v104, v73, v47 op_sel_hi:[0,1]\n"
    "v_pk_mul_f16 v105, v73, v48 op_sel_hi:[0,1]\n"
    "v_pk_mul_f16 v106, v73, v49 op_sel_hi:[0,1]\n"
    "v_pk_mul_f16 v107, v73, v50 op_sel_hi:[0,1]\n"
    "v_mul_f32 v9, v141, v89\n"
    "v_mul_f32 v10, v161, v90\n"
    "v_mfma_f32_32x32x16_f16 v[172:187], v[128:131], v[100:103], v[172:187]\n"
    "ds_read_b128 v[124:127], v22 offset:80\n"
    "v_pk_mul_f16 v92, v66, v38 op_sel_hi:[0,1]\n"
    "v_pk_mul_f16 v93, v66, v26 op_sel:[1,0]\n"
    "v_pk_mul_f16 v94, v66, v27 op_sel:[1,0]\n"
    "v_pk_mul_f16 v95, v66, v28 op_sel:[1,0]\n"
    "v_mfma_f32_32x32x16_f16 v[188:203], v[128:131], v[104:107], v[188:203]\n"
    "v_pk_mul_f16 v96, v73, v51 op_sel_hi:[0,1]\n"
    "v_pk_mul_f16 v97, v73, v39 op_sel:[1,0]\n"
    "v_pk_mul_f16 v98, v73, v40 op_sel:[1,0]\n"
    "v_pk_mul_f16 v99, v73, v41 op_sel:[1,0]\n"
    "v_mul_f32 v11, v157, v90\n"
    "s_waitcnt lgkmcnt(1)\n"
    "v_mfma_f32_32x32x16_f16 v[172:187], v[132:135], v[92:95], v[172:187]\n"
    "ds_read_b128 v[128:131], v22 offset:96\n"
    "v_pk_mul_f16 v100, v66, v29 op_sel:[1,0]\n"
    "v_pk_mul_f16 v101, v66, v30 op_sel:[1,0]\n"
    "v_pk_mul_f16 v102, v66, v31 op_sel:[1,0]\n"
    "v_pk_mul_f16 v103, v66, v32 op_sel:[1,0]\n"
    "v_mfma_f32_32x32x16_f16 v[188:203], v[132:135], v[96:99], v[188:203]\n"
    "v_pk_mul_f16 v104, v73, v42 op_sel:[1,0]\n"
    "v_pk_mul_f16 v105, v73, v43 op_sel:[1,0]\n"
    "v_pk_mul_f16 v106, v73, v44 op_sel:[1,0]\n"
    "v_pk_mul_f16 v107, v73, v45 op_sel:[1,0]\n"
    "v_cvt_pk_f16_f32 v67, v8, v9\n"
    "v_cvt_pk_f16_f32 v74, v10, v11\n"
    "v_mfma_f32_32x32x16_f16 v[172:187], v[136:139], v[100:103], v[172:187]\n"
    "ds_read_b128 v[132:135], v22 offset:112\n"
    "v_pk_mul_f16 v92, v66, v33 op_sel:[1,0]\n"
    "v_pk_mul_f16 v93, v66, v34 op_sel:[1,0]\n"
    "v_pk_mul_f16 v94, v66, v35 op_sel:[1,0]\n"
    "v_pk_mul_f16 v95, v66, v36 op_sel:[1,0]\n"
    "v_mfma_f32_32x32x16_f16 v[188:203], v[136:139], v[104:107], v[188:203]\n"
    "v_pk_mul_f16 v96, v73, v46 op_sel:[1,0]\n"
    "v_pk_mul_f16 v97, v73, v47 op_sel:[1,0]\n"
    "v_pk_mul_f16 v98, v73, v48 op_sel:[1,0]\n"
    "v_pk_mul_f16 v99, v73, v49 op_sel:[1,0]\n"
    "v_mul_f32 v8, v146, v89\n"
    "s_waitcnt lgkmcnt(1)\n"
    "v_mfma_f32_32x32x16_f16 v[172:187], v[124:127], v[92:95], v[172:187]\n"
    "ds_read_b128 v[136:139], v22 offset:128\n"
    "v_pk_mul_f16 v100, v66, v37 op_sel:[1,0]\n"
    "v_pk_mul_f16 v101, v66, v38 op_sel:[1,0]\n"
    "v_pk_mul_f16 v102, v67, v26 op_sel_hi:[0,1]\n"
    "v_pk_mul_f16 v103, v67, v27 op_sel_hi:[0,1]\n"
    "v_mfma_f32_32x32x16_f16 v[188:203], v[124:127], v[96:99], v[188:203]\n"
    "v_pk_mul_f16 v104, v73, v50 op_sel:[1,0]\n"
    "v_pk_mul_f16 v105, v73, v51 op_sel:[1,0]\n"
    "v_pk_mul_f16 v106, v74, v39 op_sel_hi:[0,1]\n"
    "v_pk_mul_f16 v107, v74, v40 op_sel_hi:[0,1]\n"
    "LM0_%=:\n"
    "s_cbranch_execz LMR_%=\n"
    "v_mul_f32 v9, v147, v89\n"
    "v_mul_f32 v10, v162, v90\n"
    "v_mfma_f32_32x32x16_f16 v[172:187], v[128:131], v[100:103], v[172:187]\n"
    "ds_read_b128 v[124:127], v22 offset:144\n"
    "v_pk_mul_f16 v92, v67, v28 op_sel_hi:[0,1]\n"
    "v_pk_mul_f16 v93, v67, v29 op_sel_hi:[0,1]\n"
    "v_pk_mul_f16 v94, v67, v30 op_sel_hi:[0,1]\n"
    "v_pk_mul_f16 v95, v67, v31 op_sel_hi:[0,1]\n"
    "v_mfma_f32_32x32x16_f16 v[188:203], v[128:131], v[104:107], v[188:203]\n"
    "v_pk_mul_f16 v96, v74, v41 op_sel_hi:[0,1]\n"
    "v_pk_mul_f16 v97, v74, v42 op_sel_hi:[0,1]\n"
    "v_pk_mul_f16 v98, v74, v43 op_sel_hi:[0,1]\n"
    "v_pk_mul_f16 v99, v74, v44 op_sel_hi:[0,1]\n"
    "v_mul_f32 v11, v163, v90\n"
    "s_waitcnt lgkmcnt(1)\n"
    "v_mfma_f32_32x32x16_f16 v[172:187], v[132:135], v[92:95], v[172:187]\n"
    "ds_read_b128 v[128:131], v22 offset:160\n"
    "v_pk_mul_f16 v100, v67, v32 op_sel_hi:[0,1]\n"
    "v_pk_mul_f16 v101, v67, v33 op_sel_hi:[0,1]\n"
    "v_pk_mul_f16 v102, v67, v34 op_sel_hi:[0,1]\n"
    "v_pk_mul_f16 v103, v67, v35 op_sel_hi:[0,1]\n"
    "v_mfma_f32_32x32x16_f16 v[188:203], v[132:135], v[96:99], v[188:203]\n"
    "v_pk_mul_f16 v104, v74, v45 op_sel_hi:[0,1]\n"
    "v_pk_mul_f16 v105, v74, v46 op_sel_hi:[0,1]\n"
    "v_pk_mul_f16 v106, v74, v47 op_sel_hi:[0,1]\n"
    "v_pk_mul_f16 v107, v74, v48 op_sel_hi:[0,1]\n"
    "v_cvt_pk_f16_f32 v68, v8, v9\n"
    "v_cvt_pk_f16_f32 v75, v10, v11\n"
    "v_mfma_f32_32x32x16_f16 v[172:187], v[136:139], v[100:103], v[172:187]\n"
    "ds_read_b128 v[132:135], v22 offset:176\n"
    "v_pk_mul_f16 v92, v67, v36 op_sel_hi:[0,1]\n"
    "v_pk_mul_f16 v93, v67, v37 op_sel_hi:[0,1]\n"
    "v_pk_mul_f16 v94, v67, v38 op_sel_hi:[0,1]\n"
    "v_pk_mul_f16 v95, v67, v26 op_sel:[1,0]\n"
    "v_mfma_f32_32x32x16_f16 v[188:203], v[136:139], v[104:107], v[188:203]\n"
    "v_pk_mul_f16 v96, v74, v49 op_sel_hi:[0,1]\n"
    "v_pk_mul_f16 v97, v74, v50 op_sel_hi:[0,1]\n"
    "v_pk_mul_f16 v98, v74, v51 op_sel_hi:[0,1]\n"
    "v_pk_mul_f16 v99, v74, v39 op_sel:[1,0]\n"
    "v_mul_f32 v8, v142, v89\n"
    "s_waitcnt lgkmcnt(1)\n"
    "v_mfma_f32_32x32x16_f16 v[172:187], v[124:127], v[92:95], v[172:187]\n"
    "ds_read_b128 v[136:139], v22 offset:192\n"
    "v_pk_mul_f16 v100, v67, v27 op_sel:[1,0]\n"
    "v_pk_mul_f16 v101, v67, v28 op_sel:[1,0]\n"
    "v_pk_mul_f16 v102, v67, v29 op_sel:[1,0]\n"
    "v_pk_mul_f16 v103, v67, v30 op_sel:[1,0]\n"
    "v_mfma_f32_32x32x16_f16 v[188:203], v[124:127], v[96:99], v[188:203]\n"
    "v_pk_mul_f16 v104, v74, v40 op_sel:[1,0]\n"
    "v_pk_mul_f16 v105, v74, v41 op_sel:[1,0]\n"
    "v_pk_mul_f16 v106, v74, v42 op_sel:[1,0]\n"
    "v_pk_mul_f16 v107, v74, v43 op_sel:[1,0]\n"
    "v_mul_f32 v9, v148, v89\n"
    "v_mul_f32 v10, v158, v90\n"
    "v_mfma_f32_32x32x16_f16 v[172:187], v[128:131], v[100:103], v[172:187]\n"
    "ds_read_b128 v[124:127], v22 offset:208\n"
    "v_pk_mul_f16 v92, v67, v31 op_sel:[1,0]\n"
    "v_pk_mul_f16 v93, v67, v32 op_sel:[1,0]\n"
    "v_pk_mul_f16 v94, v67, v33 op_sel:[1,0]\n"
    "v_pk_mul_f16 v95, v67, v34 op_sel:[1,0]\n"
    "v_mfma_f32_32x32x16_f16 v[188:203], v[128:131], v[104:107], v[188:203]\n"
    "v_pk_mul_f16 v96, v74, v44 op_sel:[1,0]\n"
    "v_pk_mul_f16 v97, v74, v45 op_sel:[1,0]\n"
    "v_pk_mul_f16 v98, v74, v46 op_sel:[1,0]\n"
    "v_pk_mul_f16 v99, v74, v47 op_sel:[1,0]\n"
    "v_mul_f32 v11, v164, v90\n"
    "s_waitcnt lgkmcnt(1)\n"
    "v_mfma_f32_32x32x16_f16 v[172:187], v[132:135], v[92:95], v[172:187]\n"
    "ds_read_b128 v[128:131], v22 offset:224\n"
    "v_pk_mul_f16 v100, v67, v35 op_sel:[1,0]\n"
    "v_pk_mul_f16 v101, v67, v36 op_sel:[1,0]\n"
    "v_pk_mul_f16 v102, v67, v37 op_sel:[1,0]\n"
    "v_pk_mul_f16 v103, v67, v38 op_sel:[1,0]\n"
    "v_mfma_f32_32x32x16_f16 v[188:203], v[132:135], v[96:99], v[188:203]\n"
    "v_pk_mul_f16 v104, v74, v48 op_sel:[1,0]\n"
    "v_pk_mul_f16 v105, v74, v49 op_sel:[1,0]\n"
    "v_pk_mul_f16 v106, v74, v50 op_sel:[1,0]\n"
    "v_pk_mul_f16 v107, v74, v51 op_sel:[1,0]\n"
    "v_cvt_pk_f16_f32 v69, v8, v9\n"
    "v_cvt_pk_f16_f32 v76, v10, v11\n"
    "v_mfma_f32_32x32x16_f16 v[172:187], v[136:139], v[100:103], v[172:187]\n"
    "ds_read_b128 v[132:135], v22 offset:240\n"
    "v_pk_mul_f16 v92, v68, v26 op_sel_hi:[0,1]\n"
    "v_pk_mul_f16 v93, v68, v27 op_sel_hi:[0,1]\n"
    "v_pk_mul_f16 v94, v68, v28 op_sel_hi:[0,1]\n"
    "v_pk_mul_f16 v95, v68, v29 op_sel_hi:[0,1]\n"
    "v_mfma_f32_32x32x16_f16 v[188:203], v[136:139], v[104:107], v[188:203]\n"
    "v_pk_mul_f16 v96, v75, v39 op_sel_hi:[0,1]\n"
    "v_pk_mul_f16 v97, v75, v40 op_sel_hi:[0,1]\n"
    "v_pk_mul_f16 v98, v75, v41 op_sel_hi:[0,1]\n"
    "v_pk_mul_f16 v99, v75, v42 op_sel_hi:[0,1]\n"
    "v_mul_f32 v8, v149, v89\n"
    "s_waitcnt lgkmcnt(1)\n"
    "v_mfma_f32_32x32x16_f16 v[172:187], v[124:127], v[92:95], v[172:187]\n"
    "ds_read_b128 v[136:139], v22 offset:256\n"
    "v_pk_mul_f16 v100, v68, v30 op_sel_hi:[0,1]\n"
    "v_pk_mul_f16 v101, v68, v31 op_sel_hi:[0,1]\n"
    "v_pk_mul_f16 v102, v68, v32 op_sel_hi:[0,1]\n"
    "v_pk_mul_f16 v103, v68, v33 op_sel_hi:[0,1]\n"
    "v_mfma_f32_32x32x16_f16 v[188:203], v[124:127], v[96:99], v[188:203]\n"
    "v_pk_mul_f16 v104, v75, v43 op_sel_hi:[0,1]\n"
    "v_pk_mul_f16 v105, v75, v44 op_sel_hi:[0,1]\n"
    "v_pk_mul_f16 v106, v75, v45 op_sel_hi:[0,1]\n"
    "v_pk_mul_f16 v107, v75, v46 op_sel_hi:[0,1]\n"
    "v_mul_f32 v9, v143, v89\n"
    "v_mul_f32 v10, v165, v90\n"
    "v_mfma_f32_32x32x16_f16 v[172:187], v[128:131], v[100:103], v[172:187]\n"
    "ds_read_b128 v[124:127], v22 offset:272\n"
    "v_pk_mul_f16 v92, v68, v34 op_sel_hi:[0,1]\n"
    "v_pk_mul_f16 v93, v68, v35 op_sel_hi:[0,1]\n"
    "v_pk_mul_f16 v94, v68, v36 op_sel_hi:[0,1]\n"
    "v_pk_mul_f16 v95, v68, v37 op_sel_hi:[0,1]\n"
    "v_mfma_f32_32x32x16_f16 v[188:203], v[128:131], v[104:107], v[188:203]\n"
    "v_pk_mul_f16 v96, v75, v47 op_sel_hi:[0,1]\n"
    "v_pk_mul_f16 v97, v75, v48 op_sel_hi:[0,1]\n"
    "v_pk_mul_f16 v98, v75, v49 op_sel_hi:[0,1]\n"
    "v_pk_mul_f16 v99, v75, v50 op_sel_hi:[0,1]\n"
    "v_mul_f32 v11, v159, v90\n"
    "s_waitcnt lgkmcnt(1)\n"
    "v_mfma_f32_32x32x16_f16 v[172:187], v[132:135], v[92:95], v[172:187]\n"
    "ds_read_b128 v[128:131], v22 offset:288\n"
    "v_pk_mul_f16 v100, v68, v38 op_sel_hi:[0,1]\n"
    "v_pk_mul_f16 v101, v68, v26 op_sel:[1,0]\n"
    "v_pk_mul_f16 v102, v68, v27 op_sel:[1,0]\n"
    "v_pk_mul_f16 v103, v68, v28 op_sel:[1,0]\n"
    "v_mfma_f32_32x32x16_f16 v[188:203], v[132:135], v[96:99], v[188:203]\n"
    "v_pk_mul_f16 v104, v75, v51 op_sel_hi:[0,1]\n"
    "v_pk_mul_f16 v105, v75, v39 op_sel:[1,0]\n"
    "v_pk_mul_f16 v106, v75, v40 op_sel:[1,0]\n"
    "v_pk_mul_f16 v107, v75, v41 op_sel:[1,0]\n"
    "v_cvt_pk_f16_f32 v70, v8, v9\n"
    "v_cvt_pk_f16_f32 v77, v10, v11\n"
    "v_mfma_f32_32x32x16_f16 v[172:187], v[136:139], v[100:103], v[172:187]\n"
    "ds_read_b128 v[132:135], v22 offset:304\n"
    "v_pk_mul_f16 v92, v68, v29 op_sel:[1,0]\n"
    "v_pk_mul_f16 v93, v68, v30 op_sel:[1,0]\n"
    "v_pk_mul_f16 v94, v68, v31 op_sel:[1,0]\n"
    "v_pk_mul_f16 v95, v68, v32 op_sel:[1,0]\n"
    "v_mfma_f32_32x32x16_f16 v[188:203], v[136:139], v[104:107], v[188:203]\n"
    "v_pk_mul_f16 v96, v75, v42 op_sel:[1,0]\n"
    "v_pk_mul_f16 v97, v75, v43 op_sel:[1,0]\n"
    "v_pk_mul_f16 v98, v75, v44 op_sel:[1,0]\n"
    "v_pk_mul_f16 v99, v75, v45 op_sel:[1,0]\n"
    "v_mul_f32 v8, v150, v89\n"
    "s_waitcnt lgkmcnt(1)\n"
    "v_mfma_f32_32x32x16_f16 v[172:187], v[124:127], v[92:95], v[172:187]\n"
    "ds_read_b128 v[136:139], v22 offset:320\n"
    "v_pk_mul_f16 v100, v68, v33 op_sel:[1,0]\n"
    "v_pk_mul_f16 v101, v68, v34 op_sel:[1,0]\n"
    "v_pk_mul_f16 v102, v68, v35 op_sel:[1,0]\n"
    "v_pk_mul_f16 v103, v68, v36 op_sel:[1,0]\n"
    "v_mfma_f32_32x32x16_f16 v[188:203], v[124:127], v[96:99], v[188:203]\n"
    "v_pk_mul_f16 v104, v75, v46 op_sel:[1,0]\n"
    "v_pk_mul_f16 v105, v75, v47 op_sel:[1,0]\n"
    "v_pk_mul_f16 v106, v75, v48 op_sel:[1,0]\n"
    "v_pk_mul_f16 v107, v75, v49 op_sel:[1,0]\n"
    "v_mul_f32 v9, v151, v89\n"
    "v_mul_f32 v10, v166, v90\n"
    "v_mfma_f32_32x32x16_f16 v[172:187], v[128:131], v[100:103], v[172:187]\n"
    "ds_read_b128 v[124:127], v22 offset:336\n"
    "v_pk_mul_f16 v92, v68, v37 op_sel:[1,0]\n"
    "v_pk_mul_f16 v93, v68, v38 op_sel:[1,0]\n"
    "v_pk_mul_f16 v94, v69, v26 op_sel_hi:[0,1]\n"
    "v_pk_mul_f16 v95, v69, v27 op_sel_hi:[0,1]\n"
    "v_mfma_f32_32x32x16_f16 v[188:203], v[128:131], v[104:107], v[188:203]\n"
    "v_pk_mul_f16 v96, v75, v50 op_sel:[1,0]\n"
    "v_pk_mul_f16 v97, v75, v51 op_sel:[1,0]\n"
    "v_pk_mul_f16 v98, v76, v39 op_sel_hi:[0,1]\n"
    "v_pk_mul_f16 v99, v76, v40 op_sel_hi:[0,1]\n"
    "v_mul_f32 v11, v167, v90\n"
    "s_waitcnt lgkmcnt(1)\n"
    "v_mfma_f32_32x32x16_f16 v[172:187], v[132:135], v[92:95], v[172:187]\n"
    "ds_read_b128 v[128:131], v22 offset:352\n"
    "v_pk_mul_f16 v100, v69, v28 op_sel_hi:[0,1]\n"
    "v_pk_mul_f16 v101, v69, v29 op_sel_hi:[0,1]\n"
    "v_pk_mul_f16 v102, v69, v30 op_sel_hi:[0,1]\n"
    "v_pk_mul_f16 v103, v69, v31 op_sel_hi:[0,1]\n"
    "v_mfma_f32_32x32x16_f16 v[188:203], v[132:135], v[96:99], v[188:203]\n"
    "v_pk_mul_f16 v104, v76, v41 op_sel_hi:[0,1]\n"
    "v_pk_mul_f16 v105, v76, v42 op_sel_hi:[0,1]\n"
    "v_pk_mul_f16 v106, v76, v43 op_sel_hi:[0,1]\n"
    "v_pk_mul_f16 v107, v76, v44 op_sel_hi:[0,1]\n"
    "v_cvt_pk_f16_f32 v71, v8, v9\n"
    "v_cvt_pk_f16_f32 v78, v10, v11\n"
    "v_mfma_f32_32x32x16_f16 v[172:187], v[136:139], v[100:103], v[172:187]\n"
    "ds_read_b128 v[132:135], v22 offset:368\n"
    "v_pk_mul_f16 v92, v69, v32 op_sel_hi:[0,1]\n"
    "v_pk_mul_f16 v93, v69, v33 op_sel_hi:[0,1]\n"
    "v_pk_mul_f16 v94, v69, v34 op_sel_hi:[0,1]\n"
    "v_pk_mul_f16 v95, v69, v35 op_sel_hi:[0,1]\n"
    "v_mfma_f32_32x32x16_f16 v[188:203], v[136:139], v[104:107], v[188:203]\n"
    "v_pk_mul_f16 v96, v76, v45 op_sel_hi:[0,1]\n"
    "v_pk_mul_f16 v97, v76, v46 op_sel_hi:[0,1]\n"
    "v_pk_mul_f16 v98, v76, v47 op_sel_hi:[0,1]\n"
    "v_pk_mul_f16 v99, v76, v48 op_sel_hi:[0,1]\n"
    "v_mul_f32 v8, v152, v89\n"
    "s_waitcnt lgkmcnt(1)\n"
    "v_mfma_f32_32x32x16_f16 v[172:187], v[124:127], v[92:95], v[172:187]\n"
    "ds_read_b128 v[136:139], v22 offset:384\n"
    "v_pk_mul_f16 v100, v69, v36 op_sel_hi:[0,1]\n"
    "v_pk_mul_f16 v101, v69, v37 op_sel_hi:[0,1]\n"
    "v_pk_mul_f16 v102, v69, v38 op_sel_hi:[0,1]\n"
    "v_pk_mul_f16 v103, v69, v26 op_sel:[1,0]\n"
    "v_mfma_f32_32x32x16_f16 v[188:203], v[124:127], v[96:99], v[188:203]\n"
    "v_pk_mul_f16 v104, v76, v49 op_sel_hi:[0,1]\n"
    "v_pk_mul_f16 v105, v76, v50 op_sel_hi:[0,1]\n"
    "v_pk_mul_f16 v106, v76, v51 op_sel_hi:[0,1]\n"
    "v_pk_mul_f16 v107, v76, v39 op_sel:[1,0]\n"
    "v_mul_f32 v10, v168, v90\n"
    "v_cvt_pk_f16_f32 v72, v8, v8\n"
    "v_mfma_f32_32x32x16_f16 v[172:187], v[128:131], v[100:103], v[172:187]\n"
    "ds_read_b128 v[124:127], v22 offset:400\n"
    "v_pk_mul_f16 v92, v69, v27 op_sel:[1,0]\n"
    "v_pk_mul_f16 v93, v69, v28 op_sel:[1,0]\n"
    "v_pk_mul_f16 v94, v69, v29 op_sel:[1,0]\n"
    "v_pk_mul_f16 v95, v69, v30 op_sel:[1,0]\n"
    "v_mfma_f32_32x32x16_f16 v[188:203], v[128:131], v[104:107], v[188:203]\n"
    "v_pk_mul_f16 v96, v76, v40 op_sel:[1,0]\n"
    "v_pk_mul_f16 v97, v76, v41 op_sel:[1,0]\n"
    "v_pk_mul_f16 v98, v76, v42 op_sel:[1,0]\n"
    "v_pk_mul_f16 v99, v76, v43 op_sel:[1,0]\n"
    "v_cvt_pk_f16_f32 v79, v10, v10\n"
    "s_waitcnt lgkmcnt(1)\n"
    "v_mfma_f32_32x32x16_f16 v[172:187], v[132:135], v[92:95], v[172:187]\n"
    "ds_read_b128 v[128:131], v22 offset:416\n"
    "v_pk_mul_f16 v100, v69, v31 op_sel:[1,0]\n"
    "v_pk_mul_f16 v101, v69, v32 op_sel:[1,0]\n"
    "v_pk_mul_f16 v102, v69, v33 op_sel:[1,0]\n"
    "v_pk_mul_f16 v103, v69, v34 op_sel:[1,0]\n"
    "v_mfma_f32_32x32x16_f16 v[188:203], v[132:135], v[96:99], v[188:203]\n"
    "v_pk_mul_f16 v104, v76, v44 op_sel:[1,0]\n"
    "v_pk_mul_f16 v105, v76, v45 op_sel:[1,0]\n"
    "v_pk_mul_f16 v106, v76, v46 op_sel:[1,0]\n"
    "v_pk_mul_f16 v107, v76, v47 op_sel:[1,0]\n"
    "v_mul_f32 v82, v84, v7\n"
    "v_add_u32 v20, 35840, v23\n"
    "v_mfma_f32_32x32x16_f16 v[172:187], v[136:139], v[100:103], v[172:187]\n"
    "ds_read_b128 v[132:135], v22 offset:432\n"
    "v_pk_mul_f16 v92, v69, v35 op_sel:[1,0]\n"
    "v_pk_mul_f16 v93, v69, v36 op_sel:[1,0]\n"
    "v_pk_mul_f16 v94, v69, v37 op_sel:[1,0]\n"
    "v_pk_mul_f16 v95, v69, v38 op_sel:[1,0]\n"
    "v_mfma_f32_32x32x16_f16 v[188:203], v[136:139], v[104:107], v[188:203]\n"
    "v_pk_mul_f16 v96, v76, v48 op_sel:[1,0]\n"
    "v_pk_mul_f16 v97, v76, v49 op_sel:[1,0]\n"
    "v_pk_mul_f16 v98, v76, v50 op_sel:[1,0]\n"
    "v_pk_mul_f16 v99, v76, v51 op_sel:[1,0]\n"
    "v_cndmask_b32 v20, v20, v23, s[60:61]\n"
    "s_waitcnt lgkmcnt(1)\n"
    "v_mfma_f32_32x32x16_f16 v[172:187], v[124:127], v[92:95], v[172:187]\n"
    "ds_read_b128 v[136:139], v22 offset:448\n"
    "v_pk_mul_f16 v100, v70, v26 op_sel_hi:[0,1]\n"
    "v_pk_mul_f16 v101, v70, v27 op_sel_hi:[0,1]\n"
    "v_pk_mul_f16 v102, v70, v28 op_sel_hi:[0,1]\n"
    "v_pk_mul_f16 v103, v70, v29 op_sel_hi:[0,1]\n"
    "v_mfma_f32_32x32x16_f16 v[188:203], v[124:127], v[96:99], v[188:203]\n"
    "v_pk_mul_f16 v104, v77, v39 op_sel_hi:[0,1]\n"
    "v_pk_mul_f16 v105, v77, v40 op_sel_hi:[0,1]\n"
    "v_pk_mul_f16 v106, v77, v41 op_sel_hi:[0,1]\n"
    "v_pk_mul_f16 v107, v77, v42 op_sel_hi:[0,1]\n"
    "v_mul_f32 v12, v236, v140\n"
    "v_mul_f32 v13, v236, v156\n"
    "v_mfma_f32_32x32x16_f16 v[172:187], v[128:131], v[100:103], v[172:187]\n"
    "ds_read_b128 v[124:127], v22 offset:464\n"
    "v_pk_mul_f16 v92, v70, v30 op_sel_hi:[0,1]\n"
    "v_pk_mul_f16 v93, v70, v31 op_sel_hi:[0,1]\n"
    "v_pk_mul_f16 v94, v70, v32 op_sel_hi:[0,1]\n"
    "v_pk_mul_f16 v95, v70, v33 op_sel_hi:[0,1]\n"
    "v_mfma_f32_32x32x16_f16 v[188:203], v[128:131], v[104:107], v[188:203]\n"
    "v_pk_mul_f16 v96, v77, v43 op_sel_hi:[0,1]\n"
    "v_pk_mul_f16 v97, v77, v44 op_sel_hi:[0,1]\n"
    "v_pk_mul_f16 v98, v77, v45 op_sel_hi:[0,1]\n"
    "v_pk_mul_f16 v99, v77, v46 op_sel_hi:[0,1]\n"
    "v_fmac_f32 v12, v237, v141\n"
    "s_waitcnt lgkmcnt(1)\n"
    "v_mfma_f32_32x32x16_f16 v[172:187], v[132:135], v[92:95], v[172:187]\n"
    "ds_read_b128 v[128:131], v22 offset:480\n"
    "v_pk_mul_f16 v100, v70, v34 op_sel_hi:[0,1]\n"
    "v_pk_mul_f16 v101, v70, v35 op_sel_hi:[0,1]\n"
    "v_pk_mul_f16 v102, v70, v36 op_sel_hi:[0,1]\n"
    "v_pk_mul_f16 v103, v70, v37 op_sel_hi:[0,1]\n"
    "v_mfma_f32_32x32x16_f16 v[188:203], v[132:135], v[96:99], v[188:203]\n"
    "v_pk_mul_f16 v104, v77, v47 op_sel_hi:[0,1]\n"
    "v_pk_mul_f16 v105, v77, v48 op_sel_hi:[0,1]\n"
    "v_pk_mul_f16 v106, v77, v49 op_sel_hi:[0,1]\n"
    "v_pk_mul_f16 v107, v77, v50 op_sel_hi:[0,1]\n"
    "v_fmac_f32 v13, v237, v157\n"
    "v_fmac_f32 v12, v238, v142\n"
    "v_mfma_f32_32x32x16_f16 v[172:187], v[136:139], v[100:103], v[172:187]\n"
    "ds_read_b128 v[132:135], v22 offset:496\n"
    "v_pk_mul_f16 v92, v70, v38 op_sel_hi:[0,1]\n"
    "v_pk_mul_f16 v93, v70, v26 op_sel:[1,0]\n"
    "v_pk_mul_f16 v94, v70, v27 op_sel:[1,0]\n"
    "v_pk_mul_f16 v95, v70, v28 op_sel:[1,0]\n"
    "v_mfma_f32_32x32x16_f16 v[188:203], v[136:139], v[104:107], v[188:203]\n"
    "v_pk_mul_f16 v96, v77, v51 op_sel_hi:[0,1]\n"
    "v_pk_mul_f16 v97, v77, v39 op_sel:[1,0]\n"
    "v_pk_mul_f16 v98, v77, v40 op_sel:[1,0]\n"
    "v_pk_mul_f16 v99, v77, v41 op_sel:[1,0]\n"
    "v_fmac_f32 v13, v238, v158\n"
    "s_waitcnt lgkmcnt(1)\n"
    "v_mfma_f32_32x32x16_f16 v[172:187], v[124:127], v[92:95], v[172:187]\n"
    "ds_read_b128 v[136:139], v22 offset:512\n"
    "v_pk_mul_f16 v100, v70, v29 op_sel:[1,0]\n"
    "v_pk_mul_f16 v101, v70, v30 op_sel:[1,0]\n"
    "v_pk_mul_f16 v102, v70, v31 op_sel:[1,0]\n"
    "v_pk_mul_f16 v103, v70, v32 op_sel:[1,0]\n"
    "v_mfma_f32_32x32x16_f16 v[188:203], v[124:127], v[96:99], v[188:203]\n"
    "v_pk_mul_f16 v104, v77, v42 op_sel:[1,0]\n"
    "v_pk_mul_f16 v105, v77, v43 op_sel:[1,0]\n"
    "v_pk_mul_f16 v106, v77, v44 op_sel:[1,0]\n"
    "v_pk_mul_f16 v107, v77, v45 op_sel:[1,0]\n"
    "v_fmac_f32 v12, v239, v143\n"
    "v_fmac_f32 v13, v239, v159\n"
    "v_mfma_f32_32x32x16_f16 v[172:187], v[128:131], v[100:103], v[172:187]\n"
    "ds_read_b128 v[124:127], v22 offset:528\n"
    "v_pk_mul_f16 v92, v70, v33 op_sel:[1,0]\n"
    "v_pk_mul_f16 v93, v70, v34 op_sel:[1,0]\n"
    "v_pk_mul_f16 v94, v70, v35 op_sel:[1,0]\n"
    "v_pk_mul_f16 v95, v70, v36 op_sel:[1,0]\n"
    "v_mfma_f32_32x32x16_f16 v[188:203], v[128:131], v[104:107], v[188:203]\n"
    "v_pk_mul_f16 v96, v77, v46 op_sel:[1,0]\n"
    "v_pk_mul_f16 v97, v77, v47 op_sel:[1,0]\n"
    "v_pk_mul_f16 v98, v77, v48 op_sel:[1,0]\n"
    "v_pk_mul_f16 v99, v77, v49 op_sel:[1,0]\n"
    "v_fmac_f32 v12, v240, v144\n"
    "s_waitcnt lgkmcnt(1)\n"
    "v_mfma_f32_32x32x16_f16 v[172:187], v[132:135], v[92:95], v[172:187]\n"
    "ds_read_b128 v[128:131], v22 offset:544\n"
    "v_pk_mul_f16 v100, v70, v37 op_sel:[1,0]\n"
    "v_pk_mul_f16 v101, v70, v38 op_sel:[1,0]\n"
    "v_pk_mul_f16 v102, v71, v26 op_sel_hi:[0,1]\n"
    "v_pk_mul_f16 v103, v71, v27 op_sel_hi:[0,1]\n"
    "v_mfma_f32_32x32x16_f16 v[188:203], v[132:135], v[96:99], v[188:203]\n"
    "v_pk_mul_f16 v104, v77, v50 op_sel:[1,0]\n"
    "v_pk_mul_f16 v105, v77, v51 op_sel:[1,0]\n"
    "v_pk_mul_f16 v106, v78, v39 op_sel_hi:[0,1]\n"
    "v_pk_mul_f16 v107, v78, v40 op_sel_hi:[0,1]\n"
    "v_fmac_f32 v13, v240, v160\n"
    "v_fmac_f32 v12, v241, v145\n"
    "v_mfma_f32_32x32x16_f16 v[172:187], v[136:139], v[100:103], v[172:187]\n"
    "ds_read_b128 v[132:135], v22 offset:560\n"
    "v_pk_mul_f16 v92, v71, v28 op_sel_hi:[0,1]\n"
    "v_pk_mul_f16 v93, v71, v29 op_sel_hi:[0,1]\n"
    "v_pk_mul_f16 v94, v71, v30 op_sel_hi:[0,1]\n"
    "v_pk_mul_f16 v95, v71, v31 op_sel_hi:[0,1]\n"
    "v_mfma_f32_32x32x16_f16 v[188:203], v[136:139], v[104:107], v[188:203]\n"
    "v_pk_mul_f16 v96, v78, v41 op_sel_hi:[0,1]\n"
    "v_pk_mul_f16 v97, v78, v42 op_sel_hi:[0,1]\n"
    "v_pk_mul_f16 v98, v78, v43 op_sel_hi:[0,1]\n"
    "v_pk_mul_f16 v99, v78, v44 op_sel_hi:[0,1]\n"
    "v_fmac_f32 v13, v241, v161\n"
    "s_waitcnt lgkmcnt(1)\n"
    "v_mfma_f32_32x32x16_f16 v[172:187], v[124:127], v[92:95], v[172:187]\n"
    "ds_read_b128 v[136:139], v22 offset:576\n"
    "v_pk_mul_f16 v100, v71, v32 op_sel_hi:[0,1]\n"
    "v_pk_mul_f16 v101, v71, v33 op_sel_hi:[0,1]\n"
    "v_pk_mul_f16 v102, v71, v34 op_sel_hi:[0,1]\n"
    "v_pk_mul_f16 v103, v71, v35 op_sel_hi:[0,1]\n"
    "v_mfma_f32_32x32x16_f16 v[188:203], v[124:127], v[96:99], v[188:203]\n"
    "v_pk_mul_f16 v104, v78, v45 op_sel_hi:[0,1]\n"
    "v_pk_mul_f16 v105, v78, v46 op_sel_hi:[0,1]\n"
    "v_pk_mul_f16 v106, v78, v47 op_sel_hi:[0,1]\n"
    "v_pk_mul_f16 v107, v78, v48 op_sel_hi:[0,1]\n"
    "v_fmac_f32 v12, v242, v146\n"
    "v_fmac_f32 v13, v242, v162\n"
    "v_mfma_f32_32x32x16_f16 v[172:187], v[128:131], v[100:103], v[172:187]\n"
    "ds_read_b128 v[124:127], v22 offset:592\n"
    "v_pk_mul_f16 v92, v71, v36 op_sel_hi:[0,1]\n"
    "v_pk_mul_f16 v93, v71, v37 op_sel_hi:[0,1]\n"
    "v_pk_mul_f16 v94, v71, v38 op_sel_hi:[0,1]\n"
    "v_pk_mul_f16 v95, v71, v26 op_sel:[1,0]\n"
    "v_mfma_f32_32x32x16_f16 v[188:203], v[128:131], v[104:107], v[188:203]\n"
    "v_pk_mul_f16 v96, v78, v49 op_sel_hi:[0,1]\n"
    "v_pk_mul_f16 v97, v78, v50 op_sel_hi:[0,1]\n"
    "v_pk_mul_f16 v98, v78, v51 op_sel_hi:[0,1]\n"
    "v_pk_mul_f16 v99, v78, v39 op_sel:[1,0]\n"
    "v_fmac_f32 v12, v243, v147\n"
    "s_waitcnt lgkmcnt(1)\n"
    "v_mfma_f32_32x32x16_f16 v[172:187], v[132:135], v[92:95], v[172:187]\n"
    "ds_read_b128 v[128:131], v22 offset:608\n"
    "v_pk_mul_f16 v100, v71, v27 op_sel:[1,0]\n"
    "v_pk_mul_f16 v101, v71, v28 op_sel:[1,0]\n"
    "v_pk_mul_f16 v102, v71, v29 op_sel:[1,0]\n"
    "v_pk_mul_f16 v103, v71, v30 op_sel:[1,0]\n"
    "v_mfma_f32_32x32x16_f16 v[188:203], v[132:135], v[96:99], v[188:203]\n"
    "v_pk_mul_f16 v104, v78, v40 op_sel:[1,0]\n"
    "v_pk_mul_f16 v105, v78, v41 op_sel:[1,0]\n"
    "v_pk_mul_f16 v106, v78, v42 op_sel:[1,0]\n"
    "v_pk_mul_f16 v107, v78, v43 op_sel:[1,0]\n"
    "v_fmac_f32 v13, v243, v163\n"
    "v_fmac_f32 v12, v244, v148\n"
    "v_mfma_f32_32x32x16_f16 v[172:187], v[136:139], v[100:103], v[172:187]\n"
    "ds_read_b128 v[132:135], v22 offset:624\n"
    "v_pk_mul_f16 v92, v71, v31 op_sel:[1,0]\n"
    "v_pk_mul_f16 v93, v71, v32 op_sel:[1,0]\n"
    "v_pk_mul_f16 v94, v71, v33 op_sel:[1,0]\n"
    "v_pk_mul_f16 v95, v71, v34 op_sel:[1,0]\n"
    "v_mfma_f32_32x32x16_f16 v[188:203], v[136:139], v[104:107], v[188:203]\n"
    "v_pk_mul_f16 v96, v78, v44 op_sel:[1,0]\n"
    "v_pk_mul_f16 v97, v78, v45 op_sel:[1,0]\n"
    "v_pk_mul_f16 v98, v78, v46 op_sel:[1,0]\n"
    "v_pk_mul_f16 v99, v78, v47 op_sel:[1,0]\n"
    "v_fmac_f32 v13, v244, v164\n"
    "ds_read_b32 v19, v17 offset:16\n"
    "s_waitcnt lgkmcnt(2)\n"
    "v_mfma_f32_32x32x16_f16 v[172:187], v[124:127], v[92:95], v[172:187]\n"
    "ds_read_b128 v[136:139], v22 offset:640\n"
    "v_pk_mul_f16 v100, v71, v35 op_sel:[1,0]\n"
    "v_pk_mul_f16 v101, v71, v36 op_sel:[1,0]\n"
    "v_pk_mul_f16 v102, v71, v37 op_sel:[1,0]\n"
    "v_pk_mul_f16 v103, v71, v38 op_sel:[1,0]\n"
    "v_mfma_f32_32x32x16_f16 v[188:203], v[124:127], v[96:99], v[188:203]\n"
    "v_pk_mul_f16 v104, v78, v48 op_sel:[1,0]\n"
    "v_pk_mul_f16 v105, v78, v49 op_sel:[1,0]\n"
    "v_pk_mul_f16 v106, v78, v50 op_sel:[1,0]\n"
    "v_pk_mul_f16 v107, v78, v51 op_sel:[1,0]\n"
    "v_fmac_f32 v12, v245, v149\n"
    "v_fmac_f32 v13, v245, v165\n"
    "v_mfma_f32_32x32x16_f16 v[172:187], v[128:131], v[100:103], v[172:187]\n"
    "ds_read_b128 v[124:127], v22 offset:656\n"
    "v_pk_mul_f16 v92, v72, v26 op_sel_hi:[0,1]\n"
    "v_pk_mul_f16 v93, v72, v27 op_sel_hi:[0,1]\n"
    "v_pk_mul_f16 v94, v72, v28 op_sel_hi:[0,1]\n"
    "v_pk_mul_f16 v95, v72, v29 op_sel_hi:[0,1]\n"
    "v_mfma_f32_32x32x16_f16 v[188:203], v[128:131], v[104:107], v[188:203]\n"
    "v_pk_mul_f16 v96, v79, v39 op_sel_hi:[0,1]\n"
    "v_pk_mul_f16 v97, v79, v40 op_sel_hi:[0,1]\n"
    "v_pk_mul_f16 v98, v79, v41 op_sel_hi:[0,1]\n"
    "v_pk_mul_f16 v99, v79, v42 op_sel_hi:[0,1]\n"
    "v_fmac_f32 v12, v246, v150\n"
    "s_waitcnt lgkmcnt(2)\n"
    "v_cmp_gt_u32 vcc, 2, v19\n"
    "v_fmac_f32 v13, v246, v166\n"
    "s_cbranch_vccnz L22_%=\n"
    "L21_%=:\n"
    "v_mfma_f32_32x32x16_f16 v[172:187], v[132:135], v[92:95], v[172:187]\n"
    "ds_read_b128 v[128:131], v22 offset:672\n"
    "v_pk_mul_f16 v100, v72, v30 op_sel_hi:[0,1]\n"
    "v_pk_mul_f16 v101, v72, v31 op_sel_hi:[0,1]\n"
    "v_pk_mul_f16 v102, v72, v32 op_sel_hi:[0,1]\n"
    "v_pk_mul_f16 v103, v72, v33 op_sel_hi:[0,1]\n"
    "v_mfma_f32_32x32x16_f16 v[188:203], v[132:135], v[96:99], v[188:203]\n"
    "v_pk_mul_f16 v104, v79, v43 op_sel_hi:[0,1]\n"
    "v_pk_mul_f16 v105, v79, v44 op_sel_hi:[0,1]\n"
    "v_pk_mul_f16 v106, v79, v45 op_sel_hi:[0,1]\n"
    "v_pk_mul_f16 v107, v79, v46 op_sel_hi:[0,1]\n"
    "v_fmac_f32 v12, v247, v151\n"
    "s_waitcnt lgkmcnt(1)\n"
    "v_mfma_f32_32x32x16_f16 v[172:187], v[136:139], v[100:103], v[172:187]\n"
    "ds_read_b128 v[132:135], v23 offset:0\n"
    "v_pk_mul_f16 v92, v72, v34 op_sel_hi:[0,1]\n"
    "v_pk_mul_f16 v93, v72, v35 op_sel_hi:[0,1]\n"
    "v_pk_mul_f16 v94, v72, v36 op_sel_hi:[0,1]\n"
    "v_pk_mul_f16 v95, v72, v37 op_sel_hi:[0,1]\n"
    "v_mfma_f32_32x32x16_f16 v[188:203], v[136:139], v[104:107], v[188:203]\n"
    "v_pk_mul_f16 v96, v79, v47 op_sel_hi:[0,1]\n"
    "v_pk_mul_f16 v97, v79, v48 op_sel_hi:[0,1]\n"
    "v_pk_mul_f16 v98, v79, v49 op_sel_hi:[0,1]\n"
    "v_pk_mul_f16 v99, v79, v50 op_sel_hi:[0,1]\n"
    "v_fmac_f32 v13, v247, v167\n"
    "v_fmac_f32 v12, v248, v152\n"
    "v_mfma_f32_32x32x16_f16 v[172:187], v[124:127], v[92:95], v[172:187]\n"
    "ds_read_b128 v[136:139], v23 offset:16\n"
    "v_pk_mul_f16 v100, v72, v38 op_sel_hi:[0,1]\n"
    "v_fmac_f32 v13, v248, v168\n"
    "s_nop 1\n"
    "v_permlane32_swap_b32 v12, v13\n"
    "v_mfma_f32_32x32x16_f16 v[188:203], v[124:127], v[96:99], v[188:203]\n"
    "v_pk_mul_f16 v104, v79, v51 op_sel_hi:[0,1]\n"
    "v_add_f32 v14, v12, v13\n"
    "v_fmac_f32 v85, v14, v81\n"
    "s_waitcnt lgkmcnt(1)\n"
    "v_mfma_f32_32x32x16_f16 v[172:187], v[128:131], v[100:103], v[172:187]\n"
    "ds_read_b128 v[124:127], v23 offset:32\n"
    "v_mfma_f32_32x32x16_f16 v[188:203], v[128:131], v[104:107], v[188:203]\n"
    "s_nop 9\n"
    "v_max3_f32 v8, |v172|, |v173|, |v174|\n"
    "v_max3_f32 v9, |v175|, |v176|, |v177|\n"
    "v_max3_f32 v10, |v178|, |v179|, |v180|\n"
    "v_max3_f32 v11, |v181|, |v182|, |v183|\n"
    "v_max3_f32 v8, v8, v9, |v184|\n"
    "v_max3_f32 v4, v10, v11, v8\n"
    "v_max3_f32 v8, |v188|, |v189|, |v190|\n"
    "v_max3_f32 v9, |v191|, |v192|, |v193|\n"
    "v_max3_f32 v10, |v194|, |v195|, |v196|\n"
    "v_max3_f32 v11, |v197|, |v198|, |v199|\n"
    "v_max3_f32 v8, v8, v9, |v200|\n"
    "v_max3_f32 v5, v10, v11, v8\n"
    "s_nop 1\n"
    "v_permlane32_swap_b32 v4, v5\n"
    "v_max3_f32 v4, v4, v5, v6\n"
    "v_and_b32 v4, 0x7f800000, v4\n"
    "v_sub_u32 v89, 0x82800000, v4\n"
    "v_sub_u32 v90, 0x82800000, v4\n"
    "v_subrev_u32 v7, 58720256, v4\n"
    "s_nop 0\n"
    "v_permlane32_swap_b32 v89, v90\n"
    "v_mul_f32 v8, v172, v89\n"
    "v_mul_f32 v9, v176, v89\n"
    "v_mul_f32 v10, v188, v90\n"
    "v_mul_f32 v11, v192, v90\n"
    "v_cvt_pk_f16_f32 v66, v8, v9\n"
    "v_cvt_pk_f16_f32 v73, v10, v11\n"
    "v_pk_mul_f16 v92, v66, v26 op_sel_hi:[0,1]\n"
    "v_pk_mul_f16 v93, v66, v27 op_sel_hi:[0,1]\n"
    "v_pk_mul_f16 v94, v66, v28 op_sel_hi:[0,1]\n"
    "v_pk_mul_f16 v95, v66, v29 op_sel_hi:[0,1]\n"
    "s_nop 1\n"
    "v_mfma_f32_32x32x16_f16 v[140:155], v[132:135], v[92:95], 0\n"
    "v_pk_mul_f16 v96, v73, v39 op_sel_hi:[0,1]\n"
    "v_pk_mul_f16 v97, v73, v40 op_sel_hi:[0,1]\n"
    "v_pk_mul_f16 v98, v73, v41 op_sel_hi:[0,1]\n"
    "v_pk_mul_f16 v99, v73, v42 op_sel_hi:[0,1]\n"
    "ds_read_b128 v[128:131], v23 offset:48\n"
    "v_pk_mul_f16 v100, v66, v30 op_sel_hi:[0,1]\n"
    "v_pk_mul_f16 v101, v66, v31 op_sel_hi:[0,1]\n"
    "v_pk_mul_f16 v102, v66, v32 op_sel_hi:[0,1]\n"
    "v_pk_mul_f16 v103, v66, v33 op_sel_hi:[0,1]\n"
    "v_mfma_f32_32x32x16_f16 v[156:171], v[132:135], v[96:99], 0\n"
    "v_pk_mul_f16 v104, v73, v43 op_sel_hi:[0,1]\n"
    "v_pk_mul_f16 v105, v73, v44 op_sel_hi:[0,1]\n"
    "v_pk_mul_f16 v106, v73, v45 op_sel_hi:[0,1]\n"
    "v_pk_mul_f16 v107, v73, v46 op_sel_hi:[0,1]\n"
    "v_mul_f32 v8, v177, v89\n"
    "s_waitcnt lgkmcnt(1)\n"
    "v_mfma_f32_32x32x16_f16 v[140:155], v[136:139], v[100:103], v[140:155]\n"
    "ds_read_b128 v[132:135], v23 offset:64\n"
    "v_pk_mul_f16 v92, v66, v34 op_sel_hi:[0,1]\n"
    "v_pk_mul_f16 v93, v66, v35 op_sel_hi:[0,1]\n"
    "v_pk_mul_f16 v94, v66, v36 op_sel_hi:[0,1]\n"
    "v_pk_mul_f16 v95, v66, v37 op_sel_hi:[0,1]\n"
    "v_mfma_f32_32x32x16_f16 v[156:171], v[136:139], v[104:107], v[156:171]\n"
    "v_pk_mul_f16 v96, v73, v47 op_sel_hi:[0,1]\n"
    "v_pk_mul_f16 v97, v73, v48 op_sel_hi:[0,1]\n"
    "v_pk_mul_f16 v98, v73, v49 op_sel_hi:[0,1]\n"
    "v_pk_mul_f16 v99, v73, v50 op_sel_hi:[0,1]\n"
    "v_mul_f32 v9, v173, v89\n"
    "v_mul_f32 v10, v193, v90\n"
    "v_mfma_f32_32x32x16_f16 v[140:155], v[124:127], v[92:95], v[140:155]\n"
    "ds_read_b128 v[136:139], v23 offset:80\n"
    "v_pk_mul_f16 v100, v66, v38 op_sel_hi:[0,1]\n"
    "v_pk_mul_f16 v101, v66, v26 op_sel:[1,0]\n"
    "v_pk_mul_f16 v102, v66, v27 op_sel:[1,0]\n"
    "v_pk_mul_f16 v103, v66, v28 op_sel:[1,0]\n"
    "v_mfma_f32_32x32x16_f16 v[156:171], v[124:127], v[96:99], v[156:171]\n"
    "v_pk_mul_f16 v104, v73, v51 op_sel_hi:[0,1]\n"
    "v_pk_mul_f16 v105, v73, v39 op_sel:[1,0]\n"
    "v_pk_mul_f16 v106, v73, v40 op_sel:[1,0]\n"
    "v_pk_mul_f16 v107, v73, v41 op_sel:[1,0]\n"
    "v_mul_f32 v11, v189, v90\n"
    "s_waitcnt lgkmcnt(1)\n"
    "v_mfma_f32_32x32x16_f16 v[140:155], v[128:131], v[100:103], v[140:155]\n"
    "ds_read_b128 v[124:127], v23 offset:96\n"
    "v_pk_mul_f16 v92, v66, v29 op_sel:[1,0]\n"
    "LM1_%=:\n"
    "s_cbranch_execz LMR_%=\n"
    "v_pk_mul_f16 v93, v66, v30 op_sel:[1,0]\n"
    "v_pk_mul_f16 v94, v66, v31 op_sel:[1,0]\n"
    "v_pk_mul_f16 v95, v66, v32 op_sel:[1,0]\n"
    "v_mfma_f32_32x32x16_f16 v[156:171], v[128:131], v[104:107], v[156:171]\n"
    "v_pk_mul_f16 v96, v73, v42 op_sel:[1,0]\n"
    "v_pk_mul_f16 v97, v73, v43 op_sel:[1,0]\n"
    "v_pk_mul_f16 v98, v73, v44 op_sel:[1,0]\n"
    "v_pk_mul_f16 v99, v73, v45 op_sel:[1,0]\n"
    "v_cvt_pk_f16_f32 v67, v8, v9\n"
    "v_cvt_pk_f16_f32 v74, v10, v11\n"
    "ds_read_b32 v19, v17 offset:24\n"
    "v_mfma_f32_32x32x16_f16 v[140:155], v[132:135], v[92:95], v[140:155]\n"
    "ds_read_b128 v[128:131], v23 offset:112\n"
    "v_pk_mul_f16 v100, v66, v33 op_sel:[1,0]\n"
    "v_pk_mul_f16 v101, v66, v34 op_sel:[1,0]\n"
    "v_pk_mul_f16 v102, v66, v35 op_sel:[1,0]\n"
    "v_pk_mul_f16 v103, v66, v36 op_sel:[1,0]\n"
    "v_mfma_f32_32x32x16_f16 v[156:171], v[132:135], v[96:99], v[156:171]\n"
    "v_pk_mul_f16 v104, v73, v46 op_sel:[1,0]\n"
    "v_pk_mul_f16 v105, v73, v47 op_sel:[1,0]\n"
    "v_pk_mul_f16 v106, v73, v48 op_sel:[1,0]\n"
    "v_pk_mul_f16 v107, v73, v49 op_sel:[1,0]\n"
    "v_mul_f32 v8, v178, v89\n"
    "s_waitcnt lgkmcnt(2)\n"
    "v_mfma_f32_32x32x16_f16 v[140:155], v[136:139], v[100:103], v[140:155]\n"
    "ds_read_b128 v[132:135], v23 offset:128\n"
    "v_pk_mul_f16 v92, v66, v37 op_sel:[1,0]\n"
    "v_pk_mul_f16 v93, v66, v38 op_sel:[1,0]\n"
    "v_pk_mul_f16 v94, v67, v26 op_sel_hi:[0,1]\n"
    "v_pk_mul_f16 v95, v67, v27 op_sel_hi:[0,1]\n"
    "v_mfma_f32_32x32x16_f16 v[156:171], v[136:139], v[104:107], v[156:171]\n"
    "v_pk_mul_f16 v96, v73, v50 op_sel:[1,0]\n"
    "v_pk_mul_f16 v97, v73, v51 op_sel:[1,0]\n"
    "v_pk_mul_f16 v98, v74, v39 op_sel_hi:[0,1]\n"
    "v_pk_mul_f16 v99, v74, v40 op_sel_hi:[0,1]\n"
    "v_mul_f32 v9, v179, v89\n"
    "v_mul_f32 v10, v194, v90\n"
    "s_waitcnt lgkmcnt(2)\n"
    "v_cmp_gt_u32 vcc, 2, v19\n"
    "v_mul_f32 v11, v195, v90\n"
    "s_cbranch_vccnz L25_%=\n"
    "L24_%=:\n"
    "ds_read_b128 v[236:239], v21 offset:368\n"
    "ds_read_b128 v[240:243], v21 offset:384\n"
    "v_mfma_f32_32x32x16_f16 v[140:155], v[124:127], v[92:95], v[140:155]\n"
    "ds_read_b128 v[136:139], v23 offset:144\n"
    "v_pk_mul_f16 v100, v67, v28 op_sel_hi:[0,1]\n"
    "v_pk_mul_f16 v101, v67, v29 op_sel_hi:[0,1]\n"
    "v_pk_mul_f16 v102, v67, v30 op_sel_hi:[0,1]\n"
    "v_pk_mul_f16 v103, v67, v31 op_sel_hi:[0,1]\n"
    "v_mfma_f32_32x32x16_f16 v[156:171], v[124:127], v[96:99], v[156:171]\n"
    "v_pk_mul_f16 v104, v74, v41 op_sel_hi:[0,1]\n"
    "v_pk_mul_f16 v105, v74, v42 op_sel_hi:[0,1]\n"
    "v_pk_mul_f16 v106, v74, v43 op_sel_hi:[0,1]\n"
    "v_pk_mul_f16 v107, v74, v44 op_sel_hi:[0,1]\n"
    "v_cvt_pk_f16_f32 v68, v8, v9\n"
    "s_waitcnt lgkmcnt(3)\n"
    "v_mfma_f32_32x32x16_f16 v[140:155], v[128:131], v[100:103], v[140:155]\n"
    "ds_read_b128 v[124:127], v23 offset:160\n"
    "v_pk_mul_f16 v92, v67, v32 op_sel_hi:[0,1]\n"
    "v_pk_mul_f16 v93, v67, v33 op_sel_hi:[0,1]\n"
    "v_pk_mul_f16 v94, v67, v34 op_sel_hi:[0,1]\n"
    "v_pk_mul_f16 v95, v67, v35 op_sel_hi:[0,1]\n"
    "v_mfma_f32_32x32x16_f16 v[156:171], v[128:131], v[104:107], v[156:171]\n"
    "v_pk_mul_f16 v96, v74, v45 op_sel_hi:[0,1]\n"
    "v_pk_mul_f16 v97, v74, v46 op_sel_hi:[0,1]\n"
    "v_pk_mul_f16 v98, v74, v47 op_sel_hi:[0,1]\n"
    "v_pk_mul_f16 v99, v74, v48 op_sel_hi:[0,1]\n"
    "v_cvt_pk_f16_f32 v75, v10, v11\n"
    "v_mul_f32 v8, v174, v89\n"
    "v_mfma_f32_32x32x16_f16 v[140:155], v[132:135], v[92:95], v[140:155]\n"
    "ds_read_b128 v[128:131], v23 offset:176\n"
    "v_pk_mul_f16 v100, v67, v36 op_sel_hi:[0,1]\n"
    "v_pk_mul_f16 v101, v67, v37 op_sel_hi:[0,1]\n"
    "v_pk_mul_f16 v102, v67, v38 op_sel_hi:[0,1]\n"
    "v_pk_mul_f16 v103, v67, v26 op_sel:[1,0]\n"
    "v_mfma_f32_32x32x16_f16 v[156:171], v[132:135], v[96:99], v[156:171]\n"
    "v_pk_mul_f16 v104, v74, v49 op_sel_hi:[0,1]\n"
    "v_pk_mul_f16 v105, v74, v50 op_sel_hi:[0,1]\n"
    "v_pk_mul_f16 v106, v74, v51 op_sel_hi:[0,1]\n"
    "v_pk_mul_f16 v107, v74, v39 op_sel:[1,0]\n"
    "v_mul_f32 v9, v180, v89\n"
    "s_waitcnt lgkmcnt(4)\n"
    "v_mfma_f32_32x32x16_f16 v[204:219], v[236:239], v[108:111], v[204:219]\n"
    "v_mul_f32 v10, v190, v90\n"
    "v_mul_f32 v11, v196, v90\n"
    "v_cvt_pk_f16_f32 v69, v8, v9\n"
    "v_cvt_pk_f16_f32 v76, v10, v11\n"
    "v_mul_f32 v8, v181, v89\n"
    "v_mfma_f32_32x32x16_f16 v[220:235], v[236:239], v[116:119], v[220:235]\n"
    "v_mul_f32 v9, v175, v89\n"
    "v_mul_f32 v10, v197, v90\n"
    "v_mul_f32 v11, v191, v90\n"
    "v_cvt_pk_f16_f32 v70, v8, v9\n"
    "v_cvt_pk_f16_f32 v77, v10, v11\n"
    "s_waitcnt lgkmcnt(3)\n"
    "v_mfma_f32_32x32x16_f16 v[204:219], v[240:243], v[112:115], v[204:219]\n"
    "v_mul_f32 v8, v182, v89\n"
    "v_mul_f32 v9, v183, v89\n"
    "v_mul_f32 v10, v198, v90\n"
    "v_mul_f32 v11, v199, v90\n"
    "v_cvt_pk_f16_f32 v71, v8, v9\n"
    "v_mfma_f32_32x32x16_f16 v[220:235], v[240:243], v[120:123], v[220:235]\n"
    "v_cvt_pk_f16_f32 v78, v10, v11\n"
    "v_mul_f32 v8, v184, v89\n"
    "v_mul_f32 v10, v200, v90\n"
    "v_cvt_pk_f16_f32 v72, v8, v8\n"
    "v_cvt_pk_f16_f32 v79, v10, v10\n"
    "s_waitcnt lgkmcnt(1)\n"
    "v_mfma_f32_32x32x16_f16 v[140:155], v[136:139], v[100:103], v[140:155]\n"
    "ds_read_b128 v[132:135], v23 offset:192\n"
    "v_pk_mul_f16 v92, v67, v27 op_sel:[1,0]\n"
    "v_pk_mul_f16 v93, v67, v28 op_sel:[1,0]\n"
    "v_pk_mul_f16 v94, v67, v29 op_sel:[1,0]\n"
    "v_pk_mul_f16 v95, v67, v30 op_sel:[1,0]\n"
    "v_mfma_f32_32x32x16_f16 v[156:171], v[136:139], v[104:107], v[156:171]\n"
    "v_pk_mul_f16 v96, v74, v40 op_sel:[1,0]\n"
    "v_pk_mul_f16 v97, v74, v41 op_sel:[1,0]\n"
    "v_pk_mul_f16 v98, v74, v42 op_sel:[1,0]\n"
    "v_pk_mul_f16 v99, v74, v43 op_sel:[1,0]\n"
    "v_mul_f32 v83, v82, v80\n"
    "v_mul_f32 v83, v83, v7\n"
    "v_mfma_f32_32x32x16_f16 v[140:155], v[124:127], v[92:95], v[140:155]\n"
    "ds_read_b128 v[136:139], v23 offset:208\n"
    "v_pk_mul_f16 v100, v67, v31 op_sel:[1,0]\n"
    "v_pk_mul_f16 v101, v67, v32 op_sel:[1,0]\n"
    "v_pk_mul_f16 v102, v67, v33 op_sel:[1,0]\n"
    "v_pk_mul_f16 v103, v67, v34 op_sel:[1,0]\n"
    "v_mfma_f32_32x32x16_f16 v[156:171], v[124:127], v[96:99], v[156:171]\n"
    "v_pk_mul_f16 v104, v74, v44 op_sel:[1,0]\n"
    "v_pk_mul_f16 v105, v74, v45 op_sel:[1,0]\n"
    "v_pk_mul_f16 v106, v74, v46 op_sel:[1,0]\n"
    "v_pk_mul_f16 v107, v74, v47 op_sel:[1,0]\n"
    "ds_read_b128 v[236:239], v24 offset:128\n"
    "s_waitcnt lgkmcnt(2)\n"
    "v_mfma_f32_32x32x16_f16 v[140:155], v[128:131], v[100:103], v[140:155]\n"
    "ds_read_b128 v[124:127], v23 offset:224\n"
    "v_pk_mul_f16 v92, v67, v35 op_sel:[1,0]\n"
    "v_pk_mul_f16 v93, v67, v36 op_sel:[1,0]\n"
    "v_pk_mul_f16 v94, v67, v37 op_sel:[1,0]\n"
    "v_pk_mul_f16 v95, v67, v38 op_sel:[1,0]\n"
    "v_mfma_f32_32x32x16_f16 v[156:171], v[128:131], v[104:107], v[156:171]\n"
    "v_pk_mul_f16 v96, v74, v48 op_sel:[1,0]\n"
    "v_pk_mul_f16 v97, v74, v49 op_sel:[1,0]\n"
    "v_pk_mul_f16 v98, v74, v50 op_sel:[1,0]\n"
    "v_pk_mul_f16 v99, v74, v51 op_sel:[1,0]\n"
    "ds_read_b128 v[240:243], v24 offset:144\n"
    "ds_read_b128 v[244:247], v24 offset:160\n"
    "v_mfma_f32_32x32x16_f16 v[140:155], v[132:135], v[92:95], v[140:155]\n"
    "ds_read_b128 v[128:131], v23 offset:240\n"
    "v_pk_mul_f16 v100, v68, v26 op_sel_hi:[0,1]\n"
    "v_pk_mul_f16 v101, v68, v27 op_sel_hi:[0,1]\n"
    "v_pk_mul_f16 v102, v68, v28 op_sel_hi:[0,1]\n"
    "v_pk_mul_f16 v103, v68, v29 op_sel_hi:[0,1]\n"
    "v_mfma_f32_32x32x16_f16 v[156:171], v[132:135], v[96:99], v[156:171]\n"
    "v_pk_mul_f16 v104, v75, v39 op_sel_hi:[0,1]\n"
    "v_pk_mul_f16 v105, v75, v40 op_sel_hi:[0,1]\n"
    "v_pk_mul_f16 v106, v75, v41 op_sel_hi:[0,1]\n"
    "v_pk_mul_f16 v107, v75, v42 op_sel_hi:[0,1]\n"
    "ds_read_b128 v[248:251], v24 offset:176\n"
    "s_waitcnt lgkmcnt(4)\n"
    "v_mfma_f32_32x32x16_f16 v[140:155], v[136:139], v[100:103], v[140:155]\n"
    "ds_read_b128 v[132:135], v23 offset:256\n"
    "v_pk_mul_f16 v92, v68, v30 op_sel_hi:[0,1]\n"
    "v_pk_mul_f16 v93, v68, v31 op_sel_hi:[0,1]\n"
    "v_pk_mul_f16 v94, v68, v32 op_sel_hi:[0,1]\n"
    "v_pk_mul_f16 v95, v68, v33 op_sel_hi:[0,1]\n"
    "v_mfma_f32_32x32x16_f16 v[156:171], v[136:139], v[104:107], v[156:171]\n"
    "v_pk_mul_f16 v96, v75, v43 op_sel_hi:[0,1]\n"
    "v_pk_mul_f16 v97, v75, v44 op_sel_hi:[0,1]\n"
    "v_pk_mul_f16 v98, v75, v45 op_sel_hi:[0,1]\n"
    "v_pk_mul_f16 v99, v75, v46 op_sel_hi:[0,1]\n"
    "v_mul_f32 v204, v204, v87\n"
    "v_mul_f32 v220, v220, v88\n"
    "v_mfma_f32_32x32x16_f16 v[140:155], v[124:127], v[92:95], v[140:155]\n"
    "ds_read_b128 v[136:139], v23 offset:272\n"
    "v_pk_mul_f16 v100, v68, v34 op_sel_hi:[0,1]\n"
    "v_pk_mul_f16 v101, v68, v35 op_sel_hi:[0,1]\n"
    "v_pk_mul_f16 v102, v68, v36 op_sel_hi:[0,1]\n"
    "v_pk_mul_f16 v103, v68, v37 op_sel_hi:[0,1]\n"
    "v_mfma_f32_32x32x16_f16 v[156:171], v[124:127], v[96:99], v[156:171]\n"
    "v_pk_mul_f16 v104, v75, v47 op_sel_hi:[0,1]\n"
    "v_pk_mul_f16 v105, v75, v48 op_sel_hi:[0,1]\n"
    "v_pk_mul_f16 v106, v75, v49 op_sel_hi:[0,1]\n"
    "v_pk_mul_f16 v107, v75, v50 op_sel_hi:[0,1]\n"
    "v_mul_f32 v205, v205, v87\n"
    "s_waitcnt lgkmcnt(1)\n"
    "v_mfma_f32_32x32x16_f16 v[140:155], v[128:131], v[100:103], v[140:155]\n"
    "ds_read_b128 v[124:127], v23 offset:288\n"
    "v_pk_mul_f16 v92, v68, v38 op_sel_hi:[0,1]\n"
    "v_pk_mul_f16 v93, v68, v26 op_sel:[1,0]\n"
    "v_pk_mul_f16 v94, v68, v27 op_sel:[1,0]\n"
    "v_pk_mul_f16 v95, v68, v28 op_sel:[1,0]\n"
    "v_mfma_f32_32x32x16_f16 v[156:171], v[128:131], v[104:107], v[156:171]\n"
    "v_pk_mul_f16 v96, v75, v51 op_sel_hi:[0,1]\n"
    "v_pk_mul_f16 v97, v75, v39 op_sel:[1,0]\n"
    "v_pk_mul_f16 v98, v75, v40 op_sel:[1,0]\n"
    "v_pk_mul_f16 v99, v75, v41 op_sel:[1,0]\n"
    "v_mul_f32 v221, v221, v88\n"
    "v_mul_f32 v206, v206, v87\n"
    "v_mfma_f32_32x32x16_f16 v[140:155], v[132:135], v[92:95], v[140:155]\n"
    "ds_read_b128 v[128:131], v23 offset:304\n"
    "v_pk_mul_f16 v100, v68, v29 op_sel:[1,0]\n"
    "v_pk_mul_f16 v101, v68, v30 op_sel:[1,0]\n"
    "v_pk_mul_f16 v102, v68, v31 op_sel:[1,0]\n"
    "v_pk_mul_f16 v103, v68, v32 op_sel:[1,0]\n"
    "v_mfma_f32_32x32x16_f16 v[156:171], v[132:135], v[96:99], v[156:171]\n"
    "v_pk_mul_f16 v104, v75, v42 op_sel:[1,0]\n"
    "v_pk_mul_f16 v105, v75, v43 op_sel:[1,0]\n"
    "v_pk_mul_f16 v106, v75, v44 op_sel:[1,0]\n"
    "v_pk_mul_f16 v107, v75, v45 op_sel:[1,0]\n"
    "v_mul_f32 v222, v222, v88\n"
    "s_waitcnt lgkmcnt(1)\n"
    "v_mfma_f32_32x32x16_f16 v[140:155], v[136:139], v[100:103], v[140:155]\n"
    "ds_read_b128 v[132:135], v23 offset:320\n"
    "v_pk_mul_f16 v92, v68, v33 op_sel:[1,0]\n"
    "v_pk_mul_f16 v93, v68, v34 op_sel:[1,0]\n"
    "v_pk_mul_f16 v94, v68, v35 op_sel:[1,0]\n"
    "v_pk_mul_f16 v95, v68, v36 op_sel:[1,0]\n"
    "v_mfma_f32_32x32x16_f16 v[156:171], v[136:139], v[104:107], v[156:171]\n"
    "v_pk_mul_f16 v96, v75, v46 op_sel:[1,0]\n"
    "v_pk_mul_f16 v97, v75, v47 op_sel:[1,0]\n"
    "v_pk_mul_f16 v98, v75, v48 op_sel:[1,0]\n"
    "v_pk_mul_f16 v99, v75, v49 op_sel:[1,0]\n"
    "v_mul_f32 v207, v207, v87\n"
    "v_mul_f32 v223, v223, v88\n"
    "v_mfma_f32_32x32x16_f16 v[140:155], v[124:127], v[92:95], v[140:155]\n"
    "ds_read_b128 v[136:139], v23 offset:336\n"
    "v_pk_mul_f16 v100, v68, v37 op_sel:[1,0]\n"
    "v_pk_mul_f16 v101, v68, v38 op_sel:[1,0]\n"
    "v_pk_mul_f16 v102, v69, v26 op_sel_hi:[0,1]\n"
    "v_pk_mul_f16 v103, v69, v27 op_sel_hi:[0,1]\n"
    "v_mfma_f32_32x32x16_f16 v[156:171], v[124:127], v[96:99], v[156:171]\n"
    "v_pk_mul_f16 v104, v75, v50 op_sel:[1,0]\n"
    "v_pk_mul_f16 v105, v75, v51 op_sel:[1,0]\n"
    "v_pk_mul_f16 v106, v76, v39 op_sel_hi:[0,1]\n"
    "v_pk_mul_f16 v107, v76, v40 op_sel_hi:[0,1]\n"
    "v_mul_f32 v208, v208, v87\n"
    "s_waitcnt lgkmcnt(1)\n"
    "v_mfma_f32_32x32x16_f16 v[140:155], v[128:131], v[100:103], v[140:155]\n"
    "ds_read_b128 v[124:127], v23 offset:352\n"
    "v_pk_mul_f16 v92, v69, v28 op_sel_hi:[0,1]\n"
    "v_pk_mul_f16 v93, v69, v29 op_sel_hi:[0,1]\n"
    "v_pk_mul_f16 v94, v69, v30 op_sel_hi:[0,1]\n"
    "v_pk_mul_f16 v95, v69, v31 op_sel_hi:[0,1]\n"
    "v_mfma_f32_32x32x16_f16 v[156:171], v[128:131], v[104:107], v[156:171]\n"
    "v_pk_mul_f16 v96, v76, v41 op_sel_hi:[0,1]\n"
    "v_pk_mul_f16 v97, v76, v42 op_sel_hi:[0,1]\n"
    "v_pk_mul_f16 v98, v76, v43 op_sel_hi:[0,1]\n"
    "v_pk_mul_f16 v99, v76, v44 op_sel_hi:[0,1]\n"
    "v_mul_f32 v224, v224, v88\n"
    "v_mul_f32 v209, v209, v87\n"
    "v_mfma_f32_32x32x16_f16 v[140:155], v[132:135], v[92:95], v[140:155]\n"
    "ds_read_b128 v[128:131], v23 offset:368\n"
    "v_pk_mul_f16 v100, v69, v32 op_sel_hi:[0,1]\n"
    "v_pk_mul_f16 v101, v69, v33 op_sel_hi:[0,1]\n"
    "v_pk_mul_f16 v102, v69, v34 op_sel_hi:[0,1]\n"
    "v_pk_mul_f16 v103, v69, v35 op_sel_hi:[0,1]\n"
    "v_mfma_f32_32x32x16_f16 v[156:171], v[132:135], v[96:99], v[156:171]\n"
    "v_pk_mul_f16 v104, v76, v45 op_sel_hi:[0,1]\n"
    "v_pk_mul_f16 v105, v76, v46 op_sel_hi:[0,1]\n"
    "v_pk_mul_f16 v106, v76, v47 op_sel_hi:[0,1]\n"
    "v_pk_mul_f16 v107, v76, v48 op_sel_hi:[0,1]\n"
    "v_mul_f32 v225, v225, v88\n"
    "s_waitcnt lgkmcnt(1)\n"
    "v_mfma_f32_32x32x16_f16 v[140:155], v[136:139], v[100:103], v[140:155]\n"
    "ds_read_b128 v[132:135], v21 offset:0\n"
    "v_pk_mul_f16 v92, v69, v36 op_sel_hi:[0,1]\n"
    "v_pk_mul_f16 v93, v69, v37 op_sel_hi:[0,1]\n"
    "v_pk_mul_f16 v94, v69, v38 op_sel_hi:[0,1]\n"
    "v_pk_mul_f16 v95, v69, v26 op_sel:[1,0]\n"
    "v_mfma_f32_32x32x16_f16 v[156:171], v[136:139], v[104:107], v[156:171]\n"
    "v_pk_mul_f16 v96, v76, v49 op_sel_hi:[0,1]\n"
    "v_pk_mul_f16 v97, v76, v50 op_sel_hi:[0,1]\n"
    "v_pk_mul_f16 v98, v76, v51 op_sel_hi:[0,1]\n"
    "v_pk_mul_f16 v99, v76, v39 op_sel:[1,0]\n"
    "v_mul_f32 v210, v210, v87\n"
    "v_mul_f32 v226, v226, v88\n"
    "v_mfma_f32_32x32x16_f16 v[140:155], v[124:127], v[92:95], v[140:155]\n"
    "ds_read_b128 v[136:139], v21 offset:16\n"
    "v_pk_mul_f16 v100, v69, v27 op_sel:[1,0]\n"
    "v_pk_mul_f16 v101, v69, v28 op_sel:[1,0]\n"
    "v_pk_mul_f16 v102, v69, v29 op_sel:[1,0]\n"
    "v_pk_mul_f16 v103, v69, v30 op_sel:[1,0]\n"
    "v_mfma_f32_32x32x16_f16 v[156:171], v[124:127], v[96:99], v[156:171]\n"
    "v_pk_mul_f16 v104, v76, v40 op_sel:[1,0]\n"
    "v_pk_mul_f16 v105, v76, v41 op_sel:[1,0]\n"
    "v_pk_mul_f16 v106, v76, v42 op_sel:[1,0]\n"
    "v_pk_mul_f16 v107, v76, v43 op_sel:[1,0]\n"
    "v_mul_f32 v211, v211, v87\n"
    "s_waitcnt lgkmcnt(1)\n"
    "v_mfma_f32_32x32x16_f16 v[140:155], v[128:131], v[100:103], v[140:155]\n"
    "ds_read_b128 v[124:127], v21 offset:32\n"
    "v_pk_mul_f16 v92, v52, v26 op_sel_hi:[0,1]\n"
    "v_pk_mul_f16 v93, v52, v27 op_sel:[1,0]\n"
    "v_pk_mul_f16 v94, v53, v28 op_sel_hi:[0,1]\n"
    "v_pk_mul_f16 v95, v53, v29 op_sel:[1,0]\n"
    "v_mfma_f32_32x32x16_f16 v[156:171], v[128:131], v[104:107], v[156:171]\n"
    "v_pk_mul_f16 v96, v59, v39 op_sel_hi:[0,1]\n"
    "v_pk_mul_f16 v97, v59, v40 op_sel:[1,0]\n"
    "v_pk_mul_f16 v98, v60, v41 op_sel_hi:[0,1]\n"
    "v_pk_mul_f16 v99, v60, v42 op_sel:[1,0]\n"
    "v_mul_f32 v227, v227, v88\n"
    "v_mul_f32 v212, v212, v87\n"
    "v_mul_f32 v228, v228, v88\n"
    "v_mul_f32 v213, v213, v87\n"
    "v_mul_f32 v229, v229, v88\n"
    "v_mul_f32 v214, v214, v87\n"
    "v_mul_f32 v230, v230, v88\n"
    "v_mul_f32 v215, v215, v87\n"
    "v_mul_f32 v231, v231, v88\n"
    "v_mul_f32 v216, v216, v87\n"
    "v_mul_f32 v232, v232, v88\n"
    "v_mul_f32 v12, v236, v172\n"
    "v_mfma_f32_32x32x16_f16 v[204:219], v[132:135], v[92:95], v[204:219]\n"
    "ds_read_b128 v[128:131], v21 offset:48\n"
    "v_pk_mul_f16 v100, v54, v30 op_sel_hi:[0,1]\n"
    "v_pk_mul_f16 v101, v54, v31 op_sel:[1,0]\n"
    "v_pk_mul_f16 v102, v55, v32 op_sel_hi:[0,1]\n"
    "v_pk_mul_f16 v103, v55, v33 op_sel:[1,0]\n"
    "v_mfma_f32_32x32x16_f16 v[220:235], v[132:135], v[96:99], v[220:235]\n"
    "v_pk_mul_f16 v104, v61, v43 op_sel_hi:[0,1]\n"
    "v_pk_mul_f16 v105, v61, v44 op_sel:[1,0]\n"
    "v_pk_mul_f16 v106, v62, v45 op_sel_hi:[0,1]\n"
    "v_pk_mul_f16 v107, v62, v46 op_sel:[1,0]\n"
    "v_mul_f32 v13, v236, v188\n"
    "s_waitcnt lgkmcnt(1)\n"
    "v_mfma_f32_32x32x16_f16 v[204:219], v[136:139], v[100:103], v[204:219]\n"
    "ds_read_b128 v[132:135], v21 offset:64\n"
    "v_pk_mul_f16 v92, v56, v34 op_sel_hi:[0,1]\n"
    "v_pk_mul_f16 v93, v56, v35 op_sel:[1,0]\n"
    "v_pk_mul_f16 v94, v57, v36 op_sel_hi:[0,1]\n"
    "v_pk_mul_f16 v95, v57, v37 op_sel:[1,0]\n"
    "v_mfma_f32_32x32x16_f16 v[220:235], v[136:139], v[104:107], v[220:235]\n"
    "v_pk_mul_f16 v96, v63, v47 op_sel_hi:[0,1]\n"
    "v_pk_mul_f16 v97, v63, v48 op_sel:[1,0]\n"
    "v_pk_mul_f16 v98, v64, v49 op_sel_hi:[0,1]\n"
    "v_pk_mul_f16 v99, v64, v50 op_sel:[1,0]\n"
    "v_fmac_f32 v12, v237, v173\n"
    "v_fmac_f32 v13, v237, v189\n"
    "v_mfma_f32_32x32x16_f16 v[204:219], v[124:127], v[92:95], v[204:219]\n"
    "ds_read_b128 v[136:139], v21 offset:80\n"
    "v_pk_mul_f16 v100, v58, v38 op_sel_hi:[0,1]\n"
    "v_pk_mul_f16 v101, v52, v27 op_sel_hi:[0,1]\n"
    "v_pk_mul_f16 v102, v52, v28 op_sel:[1,0]\n"
    "v_pk_mul_f16 v103, v53, v29 op_sel_hi:[0,1]\n"
    "v_mfma_f32_32x32x16_f16 v[220:235], v[124:127], v[96:99], v[220:235]\n"
    "v_pk_mul_f16 v104, v65, v51 op_sel_hi:[0,1]\n"
    "v_pk_mul_f16 v105, v59, v40 op_sel_hi:[0,1]\n"
    "v_pk_mul_f16 v106, v59, v41 op_sel:[1,0]\n"
    "v_pk_mul_f16 v107, v60, v42 op_sel_hi:[0,1]\n"
    "v_fmac_f32 v12, v238, v174\n"
    "s_waitcnt lgkmcnt(1)\n"
    "v_mfma_f32_32x32x16_f16 v[204:219], v[128:131], v[100:103], v[204:219]\n"
    "ds_read_b128 v[124:127], v21 offset:96\n"
    "v_pk_mul_f16 v92, v53, v30 op_sel:[1,0]\n"
    "v_pk_mul_f16 v93, v54, v31 op_sel_hi:[0,1]\n"
    "v_pk_mul_f16 v94, v54, v32 op_sel:[1,0]\n"
    "v_pk_mul_f16 v95, v55, v33 op_sel_hi:[0,1]\n"
    "v_mfma_f32_32x32x16_f16 v[220:235], v[128:131], v[104:107], v[220:235]\n"
    "v_pk_mul_f16 v96, v60, v43 op_sel:[1,0]\n"
    "v_pk_mul_f16 v97, v61, v44 op_sel_hi:[0,1]\n"
    "v_pk_mul_f16 v98, v61, v45 op_sel:[1,0]\n"
    "v_pk_mul_f16 v99, v62, v46 op_sel_hi:[0,1]\n"
    "v_fmac_f32 v13, v238, v190\n"
    "v_fmac_f32 v12, v239, v175\n"
    "v_mfma_f32_32x32x16_f16 v[204:219], v[132:135], v[92:95], v[204:219]\n"
    "ds_read_b128 v[128:131], v21 offset:112\n"
    "v_pk_mul_f16 v100, v55, v34 op_sel:[1,0]\n"
    "v_pk_mul_f16 v101, v56, v35 op_sel_hi:[0,1]\n"
    "v_pk_mul_f16 v102, v56, v36 op_sel:[1,0]\n"
    "v_pk_mul_f16 v103, v57, v37 op_sel_hi:[0,1]\n"
    "v_mfma_f32_32x32x16_f16 v[220:235], v[132:135], v[96:99], v[220:235]\n"
    "v_pk_mul_f16 v104, v62, v47 op_sel:[1,0]\n"
    "v_pk_mul_f16 v105, v63, v48 op_sel_hi:[0,1]\n"
    "v_pk_mul_f16 v106, v63, v49 op_sel:[1,0]\n"
    "v_pk_mul_f16 v107, v64, v50 op_sel_hi:[0,1]\n"
    "v_fmac_f32 v13, v239, v191\n"
    "s_waitcnt lgkmcnt(1)\n"
    "v_mfma_f32_32x32x16_f16 v[204:219], v[136:139], v[100:103], v[204:219]\n"
    "ds_read_b128 v[132:135], v21 offset:128\n"
    "v_pk_mul_f16 v92, v57, v38 op_sel:[1,0]\n"
    "v_pk_mul_f16 v93, v58, v26 op_sel_hi:[0,1]\n"
    "v_pk_mul_f16 v94, v52, v28 op_sel_hi:[0,1]\n"
    "v_pk_mul_f16 v95, v52, v29 op_sel:[1,0]\n"
    "v_mfma_f32_32x32x16_f16 v[220:235], v[136:139], v[104:107], v[220:235]\n"
    "v_pk_mul_f16 v96, v64, v51 op_sel:[1,0]\n"
    "v_pk_mul_f16 v97, v65, v39 op_sel_hi:[0,1]\n"
    "v_pk_mul_f16 v98, v59, v41 op_sel_hi:[0,1]\n"
    "v_pk_mul_f16 v99, v59, v42 op_sel:[1,0]\n"
    "v_fmac_f32 v12, v240, v176\n"
    "v_fmac_f32 v13, v240, v192\n"
    "v_mfma_f32_32x32x16_f16 v[204:219], v[124:127], v[92:95], v[204:219]\n"
    "ds_read_b128 v[136:139], v21 offset:144\n"
    "v_pk_mul_f16 v100, v53, v30 op_sel_hi:[0,1]\n"
    "v_pk_mul_f16 v101, v53, v31 op_sel:[1,0]\n"
    "v_pk_mul_f16 v102, v54, v32 op_sel_hi:[0,1]\n"
    "v_pk_mul_f16 v103, v54, v33 op_sel:[1,0]\n"
    "v_mfma_f32_32x32x16_f16 v[220:235], v[124:127], v[96:99], v[220:235]\n"
    "v_pk_mul_f16 v104, v60, v43 op_sel_hi:[0,1]\n"
    "v_pk_mul_f16 v105, v60, v44 op_sel:[1,0]\n"
    "v_pk_mul_f16 v106, v61, v45 op_sel_hi:[0,1]\n"
    "v_pk_mul_f16 v107, v61, v46 op_sel:[1,0]\n"
    "v_fmac_f32 v12, v241, v177\n"
    "s_waitcnt lgkmcnt(1)\n"
    "v_mfma_f32_32x32x16_f16 v[204:219], v[128:131], v[100:103], v[204:219]\n"
    "ds_read_b128 v[124:127], v21 offset:160\n"
    "v_pk_mul_f16 v92, v55, v34 op_sel_hi:[0,1]\n"
    "v_pk_mul_f16 v93, v55, v35 op_sel:[1,0]\n"
    "v_pk_mul_f16 v94, v56, v36 op_sel_hi:[0,1]\n"
    "v_pk_mul_f16 v95, v56, v37 op_sel:[1,0]\n"
    "v_mfma_f32_32x32x16_f16 v[220:235], v[128:131], v[104:107], v[220:235]\n"
    "v_pk_mul_f16 v96, v62, v47 op_sel_hi:[0,1]\n"
    "v_pk_mul_f16 v97, v62, v48 op_sel:[1,0]\n"
    "v_pk_mul_f16 v98, v63, v49 op_sel_hi:[0,1]\n"
    "v_pk_mul_f16 v99, v63, v50 op_sel:[1,0]\n"
    "v_fmac_f32 v13, v241, v193\n"
    "v_fmac_f32 v12, v242, v178\n"
    "v_mfma_f32_32x32x16_f16 v[204:219], v[132:135], v[92:95], v[204:219]\n"
    "ds_read_b128 v[128:131], v21 offset:176\n"
    "v_pk_mul_f16 v100, v57, v38 op_sel_hi:[0,1]\n"
    "v_pk_mul_f16 v101, v57, v26 op_sel:[1,0]\n"
    "v_pk_mul_f16 v102, v58, v27 op_sel_hi:[0,1]\n"
    "v_pk_mul_f16 v103, v52, v29 op_sel_hi:[0,1]\n"
    "v_mfma_f32_32x32x16_f16 v[220:235], v[132:135], v[96:99], v[220:235]\n"
    "v_pk_mul_f16 v104, v64, v51 op_sel_hi:[0,1]\n"
    "v_pk_mul_f16 v105, v64, v39 op_sel:[1,0]\n"
    "v_pk_mul_f16 v106, v65, v40 op_sel_hi:[0,1]\n"
    "v_pk_mul_f16 v107, v59, v42 op_sel_hi:[0,1]\n"
    "v_fmac_f32 v13, v242, v194\n"
    "s_waitcnt lgkmcnt(1)\n"
    "v_mfma_f32_32x32x16_f16 v[204:219], v[136:139], v[100:103], v[204:219]\n"
    "ds_read_b128 v[132:135], v21 offset:192\n"
    "v_pk_mul_f16 v92, v52, v30 op_sel:[1,0]\n"
    "v_pk_mul_f16 v93, v53, v31 op_sel_hi:[0,1]\n"
    "v_pk_mul_f16 v94, v53, v32 op_sel:[1,0]\n"
    "v_pk_mul_f16 v95, v54, v33 op_sel_hi:[0,1]\n"
    "v_mfma_f32_32x32x16_f16 v[220:235], v[136:139], v[104:107], v[220:235]\n"
    "v_pk_mul_f16 v96, v59, v43 op_sel:[1,0]\n"
    "v_pk_mul_f16 v97, v60, v44 op_sel_hi:[0,1]\n"
    "v_pk_mul_f16 v98, v60, v45 op_sel:[1,0]\n"
    "v_pk_mul_f16 v99, v61, v46 op_sel_hi:[0,1]\n"
    "v_fmac_f32 v12, v243, v179\n"
    "v_fmac_f32 v13, v243, v195\n"
    "v_mfma_f32_32x32x16_f16 v[204:219], v[124:127], v[92:95], v[204:219]\n"
    "ds_read_b128 v[136:139], v21 offset:208\n"
    "v_pk_mul_f16 v100, v54, v34 op_sel:[1,0]\n"
    "v_pk_mul_f16 v101, v55, v35 op_sel_hi:[0,1]\n"
    "v_pk_mul_f16 v102, v55, v36 op_sel:[1,0]\n"
    "v_pk_mul_f16 v103, v56, v37 op_sel_hi:[0,1]\n"
    "v_mfma_f32_32x32x16_f16 v[220:235], v[124:127], v[96:99], v[220:235]\n"
    "v_pk_mul_f16 v104, v61, v47 op_sel:[1,0]\n"
    "v_pk_mul_f16 v105, v62, v48 op_sel_hi:[0,1]\n"
    "v_pk_mul_f16 v106, v62, v49 op_sel:[1,0]\n"
    "v_pk_mul_f16 v107, v63, v50 op_sel_hi:[0,1]\n"
    "v_fmac_f32 v12, v244, v180\n"
    "s_waitcnt lgkmcnt(1)\n"
    "v_mfma_f32_32x32x16_f16 v[204:219], v[128:131], v[100:103], v[204:219]\n"
    "ds_read_b128 v[124:127], v21 offset:224\n"
    "v_pk_mul_f16 v92, v56, v38 op_sel:[1,0]\n"
    "v_pk_mul_f16 v93, v57, v26 op_sel_hi:[0,1]\n"
    "v_pk_mul_f16 v94, v57, v27 op_sel:[1,0]\n"
    "v_pk_mul_f16 v95, v58, v28 op_sel_hi:[0,1]\n"
    "v_mfma_f32_32x32x16_f16 v[220:235], v[128:131], v[104:107], v[220:235]\n"
    "v_pk_mul_f16 v96, v63, v51 op_sel:[1,0]\n"
    "v_pk_mul_f16 v97, v64, v39 op_sel_hi:[0,1]\n"
    "v_pk_mul_f16 v98, v64, v40 op_sel:[1,0]\n"
    "v_pk_mul_f16 v99, v65, v41 op_sel_hi:[0,1]\n"
    "v_fmac_f32 v13, v244, v196\n"
    "v_fmac_f32 v12, v245, v181\n"
    "v_mfma_f32_32x32x16_f16 v[204:219], v[132:135], v[92:95], v[204:219]\n"
    "ds_read_b128 v[128:131], v21 offset:240\n"
    "v_pk_mul_f16 v100, v52, v30 op_sel_hi:[0,1]\n"
    "v_pk_mul_f16 v101, v52, v31 op_sel:[1,0]\n"
    "v_pk_mul_f16 v102, v53, v32 op_sel_hi:[0,1]\n"
    "v_pk_mul_f16 v103, v53, v33 op_sel:[1,0]\n"
    "v_mfma_f32_32x32x16_f16 v[220:235], v[132:135], v[96:99], v[220:235]\n"
    "v_pk_mul_f16 v104, v59, v43 op_sel_hi:[0,1]\n"
    "v_pk_mul_f16 v105, v59, v44 op_sel:[1,0]\n"
    "v_pk_mul_f16 v106, v60, v45 op_sel_hi:[0,1]\n"
    "v_pk_mul_f16 v107, v60, v46 op_sel:[1,0]\n"
    "v_fmac_f32 v13, v245, v197\n"
    "s_waitcnt lgkmcnt(1)\n"
    "v_mfma_f32_32x32x16_f16 v[204:219], v[136:139], v[100:103], v[204:219]\n"
    "ds_read_b128 v[132:135], v21 offset:256\n"
    "v_pk_mul_f16 v92, v54, v34 op_sel_hi:[0,1]\n"
    "v_pk_mul_f16 v93, v54, v35 op_sel:[1,0]\n"
    "v_pk_mul_f16 v94, v55, v36 op_sel_hi:[0,1]\n"
    "v_pk_mul_f16 v95, v55, v37 op_sel:[1,0]\n"
    "v_mfma_f32_32x32x16_f16 v[220:235], v[136:139], v[104:107], v[220:235]\n"
    "v_pk_mul_f16 v96, v61, v47 op_sel_hi:[0,1]\n"
    "v_pk_mul_f16 v97, v61, v48 op_sel:[1,0]\n"
    "v_pk_mul_f16 v98, v62, v49 op_sel_hi:[0,1]\n"
    "v_pk_mul_f16 v99, v62, v50 op_sel:[1,0]\n"
    "v_fmac_f32 v12, v246, v182\n"
    "v_fmac_f32 v13, v246, v198\n"
    "v_mfma_f32_32x32x16_f16 v[204:219], v[124:127], v[92:95], v[204:219]\n"
    "ds_read_b128 v[136:139], v21 offset:272\n"
    "v_pk_mul_f16 v100, v56, v38 op_sel_hi:[0,1]\n"
    "v_pk_mul_f16 v101, v56, v26 op_sel:[1,0]\n"
    "v_pk_mul_f16 v102, v57, v27 op_sel_hi:[0,1]\n"
    "v_pk_mul_f16 v103, v57, v28 op_sel:[1,0]\n"
    "v_mfma_f32_32x32x16_f16 v[220:235], v[124:127], v[96:99], v[220:235]\n"
    "v_pk_mul_f16 v104, v63, v51 op_sel_hi:[0,1]\n"
    "v_pk_mul_f16 v105, v63, v39 op_sel:[1,0]\n"
    "v_pk_mul_f16 v106, v64, v40 op_sel_hi:[0,1]\n"
    "v_pk_mul_f16 v107, v64, v41 op_sel:[1,0]\n"
    "v_fmac_f32 v12, v247, v183\n"
    "s_waitcnt lgkmcnt(1)\n"
    "v_mfma_f32_32x32x16_f16 v[204:219], v[128:131], v[100:103], v[204:219]\n"
    "ds_read_b128 v[124:127], v21 offset:288\n"
    "v_pk_mul_f16 v92, v58, v29 op_sel_hi:[0,1]\n"
    "v_pk_mul_f16 v93, v52, v31 op_sel_hi:[0,1]\n"
    "v_pk_mul_f16 v94, v52, v32 op_sel:[1,0]\n"
    "v_pk_mul_f16 v95, v53, v33 op_sel_hi:[0,1]\n"
    "v_mfma_f32_32x32x16_f16 v[220:235], v[128:131], v[104:107], v[220:235]\n"
    "v_pk_mul_f16 v96, v65, v42 op_sel_hi:[0,1]\n"
    "v_pk_mul_f16 v97, v59, v44 op_sel_hi:[0,1]\n"
    "v_pk_mul_f16 v98, v59, v45 op_sel:[1,0]\n"
    "v_pk_mul_f16 v99, v60, v46 op_sel_hi:[0,1]\n"
    "v_fmac_f32 v13, v247, v199\n"
    "v_fmac_f32 v12, v248, v184\n"
    "v_mfma_f32_32x32x16_f16 v[204:219], v[132:135], v[92:95], v[204:219]\n"
    "ds_read_b128 v[128:131], v21 offset:304\n"
    "v_pk_mul_f16 v100, v53, v34 op_sel:[1,0]\n"
    "v_pk_mul_f16 v101, v54, v35 op_sel_hi:[0,1]\n"
    "v_pk_mul_f16 v102, v54, v36 op_sel:[1,0]\n"
    "v_pk_mul_f16 v103, v55, v37 op_sel_hi:[0,1]\n"
    "v_mfma_f32_32x32x16_f16 v[220:235], v[132:135], v[96:99], v[220:235]\n"
    "v_pk_mul_f16 v104, v60, v47 op_sel:[1,0]\n"
    "v_pk_mul_f16 v105, v61, v48 op_sel_hi:[0,1]\n"
    "v_pk_mul_f16 v106, v61, v49 op_sel:[1,0]\n"
    "v_pk_mul_f16 v107, v62, v50 op_sel_hi:[0,1]\n"
    "v_fmac_f32 v13, v248, v200\n"
    "s_waitcnt lgkmcnt(1)\n"
    "v_mfma_f32_32x32x16_f16 v[204:219], v[136:139], v[100:103], v[204:219]\n"
    "ds_read_b128 v[132:135], v21 offset:320\n"
    "v_pk_mul_f16 v92, v55, v38 op_sel:[1,0]\n"
    "v_pk_mul_f16 v93, v56, v26 op_sel_hi:[0,1]\n"
    "v_pk_mul_f16 v94, v56, v27 op_sel:[1,0]\n"
    "v_pk_mul_f16 v95, v57, v28 op_sel_hi:[0,1]\n"
    "v_mfma_f32_32x32x16_f16 v[220:235], v[136:139], v[104:107], v[220:235]\n"
    "v_pk_mul_f16 v96, v62, v51 op_sel:[1,0]\n"
    "v_pk_mul_f16 v97, v63, v39 op_sel_hi:[0,1]\n"
    "v_pk_mul_f16 v98, v63, v40 op_sel:[1,0]\n"
    "v_pk_mul_f16 v99, v64, v41 op_sel_hi:[0,1]\n"
    "v_permlane32_swap_b32 v12, v13\n"
    "v_add_f32 v14, v12, v13\n"
    "v_mfma_f32_32x32x16_f16 v[204:219], v[124:127], v[92:95], v[204:219]\n"
    "ds_read_b128 v[136:139], v21 offset:336\n"
    "v_pk_mul_f16 v100, v57, v29 op_sel:[1,0]\n"
    "v_pk_mul_f16 v101, v58, v30 op_sel_hi:[0,1]\n"
    "v_pk_mul_f16 v102, v52, v32 op_sel_hi:[0,1]\n"
    "v_pk_mul_f16 v103, v52, v33 op_sel:[1,0]\n"
    "v_mfma_f32_32x32x16_f16 v[220:235], v[124:127], v[96:99], v[220:235]\n"
    "v_pk_mul_f16 v104, v64, v42 op_sel:[1,0]\n"
    "v_pk_mul_f16 v105, v65, v43 op_sel_hi:[0,1]\n"
    "v_pk_mul_f16 v106, v59, v45 op_sel_hi:[0,1]\n"
    "v_pk_mul_f16 v107, v59, v46 op_sel:[1,0]\n"
    "v_fmac_f32 v85, v14, v82\n"
    "s_waitcnt lgkmcnt(1)\n"
    "v_mfma_f32_32x32x16_f16 v[204:219], v[128:131], v[100:103], v[204:219]\n"
    "ds_read_b128 v[124:127], v21 offset:352\n"
    "LM2_%=:\n"
    "s_cbranch_execz LMR_%=\n"
    "v_pk_mul_f16 v92, v53, v34 op_sel_hi:[0,1]\n"
    "v_pk_mul_f16 v93, v53, v35 op_sel:[1,0]\n"
    "v_pk_mul_f16 v94, v54, v36 op_sel_hi:[0,1]\n"
    "v_pk_mul_f16 v95, v54, v37 op_sel:[1,0]\n"
    "v_mfma_f32_32x32x16_f16 v[220:235], v[128:131], v[104:107], v[220:235]\n"
    "v_pk_mul_f16 v96, v60, v47 op_sel_hi:[0,1]\n"
    "v_pk_mul_f16 v97, v60, v48 op_sel:[1,0]\n"
    "v_pk_mul_f16 v98, v61, v49 op_sel_hi:[0,1]\n"
    "v_pk_mul_f16 v99, v61, v50 op_sel:[1,0]\n"
    "ds_read_b128 v[236:239], v24 offset:256\n"
    "ds_read_b128 v[240:243], v24 offset:272\n"
    "v_mfma_f32_32x32x16_f16 v[204:219], v[132:135], v[92:95], v[204:219]\n"
    "ds_read_b128 v[128:131], v23 offset:384\n"
    "v_pk_mul_f16 v100, v55, v38 op_sel_hi:[0,1]\n"
    "v_pk_mul_f16 v101, v55, v26 op_sel:[1,0]\n"
    "v_pk_mul_f16 v102, v56, v27 op_sel_hi:[0,1]\n"
    "v_pk_mul_f16 v103, v56, v28 op_sel:[1,0]\n"
    "v_mfma_f32_32x32x16_f16 v[220:235], v[132:135], v[96:99], v[220:235]\n"
    "v_pk_mul_f16 v104, v62, v51 op_sel_hi:[0,1]\n"
    "v_pk_mul_f16 v105, v62, v39 op_sel:[1,0]\n"
    "v_pk_mul_f16 v106, v63, v40 op_sel_hi:[0,1]\n"
    "v_pk_mul_f16 v107, v63, v41 op_sel:[1,0]\n"
    "ds_read_b128 v[244:247], v24 offset:288\n"
    "s_waitcnt lgkmcnt(4)\n"
    "v_mfma_f32_32x32x16_f16 v[204:219], v[136:139], v[100:103], v[204:219]\n"
    "ds_read_b128 v[132:135], v23 offset:400\n"
    "v_pk_mul_f16 v92, v57, v29 op_sel_hi:[0,1]\n"
    "v_pk_mul_f16 v93, v57, v30 op_sel:[1,0]\n"
    "v_pk_mul_f16 v94, v58, v31 op_sel_hi:[0,1]\n"
    "ds_read_b128 v[248:251], v24 offset:304\n"
    "v_mfma_f32_32x32x16_f16 v[220:235], v[136:139], v[104:107], v[220:235]\n"
    "v_pk_mul_f16 v96, v64, v42 op_sel_hi:[0,1]\n"
    "v_pk_mul_f16 v97, v64, v43 op_sel:[1,0]\n"
    "v_pk_mul_f16 v98, v65, v44 op_sel_hi:[0,1]\n"
    "v_mfma_f32_32x32x16_f16 v[204:219], v[124:127], v[92:95], v[204:219]\n"
    "ds_read_b128 v[136:139], v23 offset:416\n"
    "v_pk_mul_f16 v100, v69, v31 op_sel:[1,0]\n"
    "v_pk_mul_f16 v101, v69, v32 op_sel:[1,0]\n"
    "v_pk_mul_f16 v102, v69, v33 op_sel:[1,0]\n"
    "v_pk_mul_f16 v103, v69, v34 op_sel:[1,0]\n"
    "v_mfma_f32_32x32x16_f16 v[220:235], v[124:127], v[96:99], v[220:235]\n"
    "v_pk_mul_f16 v104, v76, v44 op_sel:[1,0]\n"
    "v_pk_mul_f16 v105, v76, v45 op_sel:[1,0]\n"
    "v_pk_mul_f16 v106, v76, v46 op_sel:[1,0]\n"
    "v_pk_mul_f16 v107, v76, v47 op_sel:[1,0]\n"
    "s_waitcnt lgkmcnt(4)\n"
    "v_mfma_f32_32x32x16_f16 v[140:155], v[128:131], v[100:103], v[140:155]\n"
    "ds_read_b128 v[124:127], v23 offset:432\n"
    "v_pk_mul_f16 v92, v69, v35 op_sel:[1,0]\n"
    "v_pk_mul_f16 v93, v69, v36 op_sel:[1,0]\n"
    "v_pk_mul_f16 v94, v69, v37 op_sel:[1,0]\n"
    "v_pk_mul_f16 v95, v69, v38 op_sel:[1,0]\n"
    "v_mfma_f32_32x32x16_f16 v[156:171], v[128:131], v[104:107], v[156:171]\n"
    "v_pk_mul_f16 v96, v76, v48 op_sel:[1,0]\n"
    "v_pk_mul_f16 v97, v76, v49 op_sel:[1,0]\n"
    "v_pk_mul_f16 v98, v76, v50 op_sel:[1,0]\n"
    "v_pk_mul_f16 v99, v76, v51 op_sel:[1,0]\n"
    "s_waitcnt lgkmcnt(1)\n"
    "v_mfma_f32_32x32x16_f16 v[140:155], v[132:135], v[92:95], v[140:155]\n"
    "ds_read_b128 v[128:131], v23 offset:448\n"
    "v_pk_mul_f16 v100, v70, v26 op_sel_hi:[0,1]\n"
    "v_pk_mul_f16 v101, v70, v27 op_sel_hi:[0,1]\n"
    "v_pk_mul_f16 v102, v70, v28 op_sel_hi:[0,1]\n"
    "v_pk_mul_f16 v103, v70, v29 op_sel_hi:[0,1]\n"
    "v_mfma_f32_32x32x16_f16 v[156:171], v[132:135], v[96:99], v[156:171]\n"
    "v_pk_mul_f16 v104, v77, v39 op_sel_hi:[0,1]\n"
    "v_pk_mul_f16 v105, v77, v40 op_sel_hi:[0,1]\n"
    "v_pk_mul_f16 v106, v77, v41 op_sel_hi:[0,1]\n"
    "v_pk_mul_f16 v107, v77, v42 op_sel_hi:[0,1]\n"
    "v_mfma_f32_32x32x16_f16 v[140:155], v[136:139], v[100:103], v[140:155]\n"
    "ds_read_b128 v[132:135], v23 offset:464\n"
    "v_pk_mul_f16 v92, v70, v30 op_sel_hi:[0,1]\n"
    "v_pk_mul_f16 v93, v70, v31 op_sel_hi:[0,1]\n"
    "v_pk_mul_f16 v94, v70, v32 op_sel_hi:[0,1]\n"
    "v_pk_mul_f16 v95, v70, v33 op_sel_hi:[0,1]\n"
    "v_mfma_f32_32x32x16_f16 v[156:171], v[136:139], v[104:107], v[156:171]\n"
    "v_pk_mul_f16 v96, v77, v43 op_sel_hi:[0,1]\n"
    "v_pk_mul_f16 v97, v77, v44 op_sel_hi:[0,1]\n"
    "v_pk_mul_f16 v98, v77, v45 op_sel_hi:[0,1]\n"
    "v_pk_mul_f16 v99, v77, v46 op_sel_hi:[0,1]\n"
    "v_max3_f32 v4, |v204|, |v205|, |v206|\n"
    "s_waitcnt lgkmcnt(1)\n"
    "v_mfma_f32_32x32x16_f16 v[140:155], v[124:127], v[92:95], v[140:155]\n"
    "ds_read_b128 v[136:139], v23 offset:480\n"
    "v_pk_mul_f16 v100, v70, v34 op_sel_hi:[0,1]\n"
    "v_pk_mul_f16 v101, v70, v35 op_sel_hi:[0,1]\n"
    "v_pk_mul_f16 v102, v70, v36 op_sel_hi:[0,1]\n"
    "v_pk_mul_f16 v103, v70, v37 op_sel_hi:[0,1]\n"
    "v_mfma_f32_32x32x16_f16 v[156:171], v[124:127], v[96:99], v[156:171]\n"
    "v_pk_mul_f16 v104, v77, v47 op_sel_hi:[0,1]\n"
    "v_pk_mul_f16 v105, v77, v48 op_sel_hi:[0,1]\n"
    "v_pk_mul_f16 v106, v77, v49 op_sel_hi:[0,1]\n"
    "v_pk_mul_f16 v107, v77, v50 op_sel_hi:[0,1]\n"
    "v_max3_f32 v5, |v220|, |v221|, |v222|\n"
    "v_max3_f32 v4, |v207|, |v208|, v4\n"
    "v_mfma_f32_32x32x16_f16 v[140:155], v[128:131], v[100:103], v[140:155]\n"
    "ds_read_b128 v[124:127], v23 offset:496\n"
    "v_pk_mul_f16 v92, v70, v38 op_sel_hi:[0,1]\n"
    "v_pk_mul_f16 v93, v70, v26 op_sel:[1,0]\n"
    "v_pk_mul_f16 v94, v70, v27 op_sel:[1,0]\n"
    "v_pk_mul_f16 v95, v70, v28 op_sel:[1,0]\n"
    "v_mfma_f32_32x32x16_f16 v[156:171], v[128:131], v[104:107], v[156:171]\n"
    "v_pk_mul_f16 v96, v77, v51 op_sel_hi:[0,1]\n"
    "v_pk_mul_f16 v97, v77, v39 op_sel:[1,0]\n"
    "v_pk_mul_f16 v98, v77, v40 op_sel:[1,0]\n"
    "v_pk_mul_f16 v99, v77, v41 op_sel:[1,0]\n"
    "v_max3_f32 v5, |v223|, |v224|, v5\n"
    "s_waitcnt lgkmcnt(1)\n"
    "v_mfma_f32_32x32x16_f16 v[140:155], v[132:135], v[92:95], v[140:155]\n"
    "ds_read_b128 v[128:131], v23 offset:512\n"
    "v_pk_mul_f16 v100, v70, v29 op_sel:[1,0]\n"
    "v_pk_mul_f16 v101, v70, v30 op_sel:[1,0]\n"
    "v_pk_mul_f16 v102, v70, v31 op_sel:[1,0]\n"
    "v_pk_mul_f16 v103, v70, v32 op_sel:[1,0]\n"
    "v_mfma_f32_32x32x16_f16 v[156:171], v[132:135], v[96:99], v[156:171]\n"
    "v_pk_mul_f16 v104, v77, v42 op_sel:[1,0]\n"
    "v_pk_mul_f16 v105, v77, v43 op_sel:[1,0]\n"
    "v_pk_mul_f16 v106, v77, v44 op_sel:[1,0]\n"
    "v_pk_mul_f16 v107, v77, v45 op_sel:[1,0]\n"
    "v_max3_f32 v4, |v209|, |v210|, v4\n"
    "v_max3_f32 v5, |v225|, |v226|, v5\n"
    "v_mfma_f32_32x32x16_f16 v[140:155], v[136:139], v[100:103], v[140:155]\n"
    "ds_read_b128 v[132:135], v23 offset:528\n"
    "v_pk_mul_f16 v92, v70, v33 op_sel:[1,0]\n"
    "v_pk_mul_f16 v93, v70, v34 op_sel:[1,0]\n"
    "v_pk_mul_f16 v94, v70, v35 op_sel:[1,0]\n"
    "v_pk_mul_f16 v95, v70, v36 op_sel:[1,0]\n"
    "v_mfma_f32_32x32x16_f16 v[156:171], v[136:139], v[104:107], v[156:171]\n"
    "v_pk_mul_f16 v96, v77, v46 op_sel:[1,0]\n"
    "v_pk_mul_f16 v97, v77, v47 op_sel:[1,0]\n"
    "v_pk_mul_f16 v98, v77, v48 op_sel:[1,0]\n"
    "v_pk_mul_f16 v99, v77, v49 op_sel:[1,0]\n"
    "v_max3_f32 v4, |v211|, |v212|, v4\n"
    "s_waitcnt lgkmcnt(1)\n"
    "v_mfma_f32_32x32x16_f16 v[140:155], v[124:127], v[92:95], v[140:155]\n"
    "ds_read_b128 v[136:139], v23 offset:544\n"
    "v_pk_mul_f16 v100, v70, v37 op_sel:[1,0]\n"
    "v_pk_mul_f16 v101, v70, v38 op_sel:[1,0]\n"
    "v_pk_mul_f16 v102, v71, v26 op_sel_hi:[0,1]\n"
    "v_pk_mul_f16 v103, v71, v27 op_sel_hi:[0,1]\n"
    "v_mfma_f32_32x32x16_f16 v[156:171], v[124:127], v[96:99], v[156:171]\n"
    "v_pk_mul_f16 v104, v77, v50 op_sel:[1,0]\n"
    "v_pk_mul_f16 v105, v77, v51 op_sel:[1,0]\n"
    "v_pk_mul_f16 v106, v78, v39 op_sel_hi:[0,1]\n"
    "v_pk_mul_f16 v107, v78, v40 op_sel_hi:[0,1]\n"
    "v_max3_f32 v5, |v227|, |v228|, v5\n"
    "v_max3_f32 v4, |v213|, |v214|, v4\n"
    "v_mfma_f32_32x32x16_f16 v[140:155], v[128:131], v[100:103], v[140:155]\n"
    "ds_read_b128 v[124:127], v23 offset:560\n"
    "v_pk_mul_f16 v92, v71, v28 op_sel_hi:[0,1]\n"
    "v_pk_mul_f16 v93, v71, v29 op_sel_hi:[0,1]\n"
    "v_pk_mul_f16 v94, v71, v30 op_sel_hi:[0,1]\n"
    "v_pk_mul_f16 v95, v71, v31 op_sel_hi:[0,1]\n"
    "v_mfma_f32_32x32x16_f16 v[156:171], v[128:131], v[104:107], v[156:171]\n"
    "v_pk_mul_f16 v96, v78, v41 op_sel_hi:[0,1]\n"
    "v_pk_mul_f16 v97, v78, v42 op_sel_hi:[0,1]\n"
    "v_pk_mul_f16 v98, v78, v43 op_sel_hi:[0,1]\n"
    "v_pk_mul_f16 v99, v78, v44 op_sel_hi:[0,1]\n"
    "v_max3_f32 v5, |v229|, |v230|, v5\n"
    "s_waitcnt lgkmcnt(1)\n"
    "v_mfma_f32_32x32x16_f16 v[140:155], v[132:135], v[92:95], v[140:155]\n"
    "ds_read_b128 v[128:131], v23 offset:576\n"
    "v_pk_mul_f16 v100, v71, v32 op_sel_hi:[0,1]\n"
    "v_pk_mul_f16 v101, v71, v33 op_sel_hi:[0,1]\n"
    "v_pk_mul_f16 v102, v71, v34 op_sel_hi:[0,1]\n"
    "v_pk_mul_f16 v103, v71, v35 op_sel_hi:[0,1]\n"
    "v_mfma_f32_32x32x16_f16 v[156:171], v[132:135], v[96:99], v[156:171]\n"
    "v_pk_mul_f16 v104, v78, v45 op_sel_hi:[0,1]\n"
    "v_pk_mul_f16 v105, v78, v46 op_sel_hi:[0,1]\n"
    "v_pk_mul_f16 v106, v78, v47 op_sel_hi:[0,1]\n"
    "v_pk_mul_f16 v107, v78, v48 op_sel_hi:[0,1]\n"
    "v_max3_f32 v4, |v215|, |v216|, v4\n"
    "v_max3_f32 v5, |v231|, |v232|, v5\n"
    "v_mfma_f32_32x32x16_f16 v[140:155], v[136:139], v[100:103], v[140:155]\n"
    "ds_read_b128 v[132:135], v23 offset:592\n"
    "v_pk_mul_f16 v92, v71, v36 op_sel_hi:[0,1]\n"
    "v_pk_mul_f16 v93, v71, v37 op_sel_hi:[0,1]\n"
    "v_pk_mul_f16 v94, v71, v38 op_sel_hi:[0,1]\n"
    "v_pk_mul_f16 v95, v71, v26 op_sel:[1,0]\n"
    "v_mfma_f32_32x32x16_f16 v[156:171], v[136:139], v[104:107], v[156:171]\n"
    "v_pk_mul_f16 v96, v78, v49 op_sel_hi:[0,1]\n"
    "v_pk_mul_f16 v97, v78, v50 op_sel_hi:[0,1]\n"
    "v_pk_mul_f16 v98, v78, v51 op_sel_hi:[0,1]\n"
    "v_pk_mul_f16 v99, v78, v39 op_sel:[1,0]\n"
    "v_permlane32_swap_b32 v4, v5\n"
    "s_waitcnt lgkmcnt(1)\n"
    "v_mfma_f32_32x32x16_f16 v[140:155], v[124:127], v[92:95], v[140:155]\n"
    "ds_read_b128 v[136:139], v23 offset:608\n"
    "v_pk_mul_f16 v100, v71, v27 op_sel:[1,0]\n"
    "v_pk_mul_f16 v101, v71, v28 op_sel:[1,0]\n"
    "v_pk_mul_f16 v102, v71, v29 op_sel:[1,0]\n"
    "v_pk_mul_f16 v103, v71, v30 op_sel:[1,0]\n"
    "v_mfma_f32_32x32x16_f16 v[156:171], v[124:127], v[96:99], v[156:171]\n"
    "v_pk_mul_f16 v104, v78, v40 op_sel:[1,0]\n"
    "v_pk_mul_f16 v105, v78, v41 op_sel:[1,0]\n"
    "v_pk_mul_f16 v106, v78, v42 op_sel:[1,0]\n"
    "v_pk_mul_f16 v107, v78, v43 op_sel:[1,0]\n"
    "v_max3_f32 v4, v4, v5, v6\n"
    "v_and_b32 v4, 0x7f800000, v4\n"
    "v_mfma_f32_32x32x16_f16 v[140:155], v[128:131], v[100:103], v[140:155]\n"
    "ds_read_b128 v[124:127], v23 offset:624\n"
    "v_pk_mul_f16 v92, v71, v31 op_sel:[1,0]\n"
    "v_pk_mul_f16 v93, v71, v32 op_sel:[1,0]\n"
    "v_pk_mul_f16 v94, v71, v33 op_sel:[1,0]\n"
    "v_pk_mul_f16 v95, v71, v34 op_sel:[1,0]\n"
    "v_mfma_f32_32x32x16_f16 v[156:171], v[128:131], v[104:107], v[156:171]\n"
    "v_pk_mul_f16 v96, v78, v44 op_sel:[1,0]\n"
    "v_pk_mul_f16 v97, v78, v45 op_sel:[1,0]\n"
    "v_pk_mul_f16 v98, v78, v46 op_sel:[1,0]\n"
    "v_pk_mul_f16 v99, v78, v47 op_sel:[1,0]\n"
    "v_sub_u32 v89, 0x82800000, v4\n"
    "ds_read_b32 v19, v17 offset:0\n"
    "s_waitcnt lgkmcnt(2)\n"
    "v_mfma_f32_32x32x16_f16 v[140:155], v[132:135], v[92:95], v[140:155]\n"
    "ds_read_b128 v[128:131], v23 offset:640\n"
    "v_pk_mul_f16 v100, v71, v35 op_sel:[1,0]\n"
    "v_pk_mul_f16 v101, v71, v36 op_sel:[1,0]\n"
    "v_pk_mul_f16 v102, v71, v37 op_sel:[1,0]\n"
    "v_pk_mul_f16 v103, v71, v38 op_sel:[1,0]\n"
    "v_mfma_f32_32x32x16_f16 v[156:171], v[132:135], v[96:99], v[156:171]\n"
    "v_pk_mul_f16 v104, v78, v48 op_sel:[1,0]\n"
    "v_pk_mul_f16 v105, v78, v49 op_sel:[1,0]\n"
    "v_pk_mul_f16 v106, v78, v50 op_sel:[1,0]\n"
    "v_pk_mul_f16 v107, v78, v51 op_sel:[1,0]\n"
    "v_sub_u32 v90, 0x82800000, v4\n"
    "v_subrev_u32 v7, 58720256, v4\n"
    "v_mfma_f32_32x32x16_f16 v[140:155], v[136:139], v[100:103], v[140:155]\n"
    "ds_read_b128 v[132:135], v23 offset:656\n"
    "v_pk_mul_f16 v92, v72, v26 op_sel_hi:[0,1]\n"
    "v_pk_mul_f16 v93, v72, v27 op_sel_hi:[0,1]\n"
    "v_pk_mul_f16 v94, v72, v28 op_sel_hi:[0,1]\n"
    "v_pk_mul_f16 v95, v72, v29 op_sel_hi:[0,1]\n"
    "v_mfma_f32_32x32x16_f16 v[156:171], v[136:139], v[104:107], v[156:171]\n"
    "v_pk_mul_f16 v96, v79, v39 op_sel_hi:[0,1]\n"
    "v_pk_mul_f16 v97, v79, v40 op_sel_hi:[0,1]\n"
    "v_pk_mul_f16 v98, v79, v41 op_sel_hi:[0,1]\n"
    "v_pk_mul_f16 v99, v79, v42 op_sel_hi:[0,1]\n"
    "v_permlane32_swap_b32 v89, v90\n"
    "s_waitcnt lgkmcnt(2)\n"
    "v_cmp_gt_u32 vcc, 4, v19\n"
    "v_mul_f32 v8, v204, v89\n"
    "s_cbranch_vccnz L28_%=\n"
    "L27_%=:\n"
    "v_mfma_f32_32x32x16_f16 v[140:155], v[124:127], v[92:95], v[140:155]\n"
    "ds_read_b128 v[136:139], v23 offset:672\n"
    "v_pk_mul_f16 v100, v72, v30 op_sel_hi:[0,1]\n"
    "v_pk_mul_f16 v101, v72, v31 op_sel_hi:[0,1]\n"
    "v_pk_mul_f16 v102, v72, v32 op_sel_hi:[0,1]\n"
    "v_pk_mul_f16 v103, v72, v33 op_sel_hi:[0,1]\n"
    "v_mfma_f32_32x32x16_f16 v[156:171], v[124:127], v[96:99], v[156:171]\n"
    "v_pk_mul_f16 v104, v79, v43 op_sel_hi:[0,1]\n"
    "v_pk_mul_f16 v105, v79, v44 op_sel_hi:[0,1]\n"
    "v_pk_mul_f16 v106, v79, v45 op_sel_hi:[0,1]\n"
    "v_pk_mul_f16 v107, v79, v46 op_sel_hi:[0,1]\n"
    "v_mul_f32 v9, v208, v89\n"
    "s_waitcnt lgkmcnt(1)\n"
    "v_mfma_f32_32x32x16_f16 v[140:155], v[128:131], v[100:103], v[140:155]\n"
    "ds_read_b128 v[124:127], v20 offset:0\n"
    "v_pk_mul_f16 v92, v72, v34 op_sel_hi:[0,1]\n"
    "v_pk_mul_f16 v93, v72, v35 op_sel_hi:[0,1]\n"
    "v_pk_mul_f16 v94, v72, v36 op_sel_hi:[0,1]\n"
    "v_pk_mul_f16 v95, v72, v37 op_sel_hi:[0,1]\n"
    "v_mfma_f32_32x32x16_f16 v[156:171], v[128:131], v[104:107], v[156:171]\n"
    "v_pk_mul_f16 v96, v79, v47 op_sel_hi:[0,1]\n"
    "v_pk_mul_f16 v97, v79, v48 op_sel_hi:[0,1]\n"
    "v_pk_mul_f16 v98, v79, v49 op_sel_hi:[0,1]\n"
    "v_pk_mul_f16 v99, v79, v50 op_sel_hi:[0,1]\n"
    "v_mul_f32 v10, v220, v90\n"
    "v_mul_f32 v11, v224, v90\n"
    "v_mfma_f32_32x32x16_f16 v[140:155], v[132:135], v[92:95], v[140:155]\n"
    "ds_read_b128 v[128:131], v20 offset:16\n"
    "v_pk_mul_f16 v100, v72, v38 op_sel_hi:[0,1]\n"
    "v_cvt_pk_f16_f32 v52, v8, v9\n"
    "v_cvt_pk_f16_f32 v59, v10, v11\n"
    "v_mul_f32 v8, v209, v89\n"
    "v_mfma_f32_32x32x16_f16 v[156:171], v[132:135], v[96:99], v[156:171]\n"
    "v_pk_mul_f16 v104, v79, v51 op_sel_hi:[0,1]\n"
    "v_mul_f32 v9, v205, v89\n"
    "v_mul_f32 v10, v225, v90\n"
    "v_mul_f32 v11, v221, v90\n"
    "v_cvt_pk_f16_f32 v53, v8, v9\n"
    "s_waitcnt lgkmcnt(1)\n"
    "v_mfma_f32_32x32x16_f16 v[140:155], v[136:139], v[100:103], v[140:155]\n"
    "ds_read_b128 v[132:135], v20 offset:32\n"
    "v_pk_mul_f16 v92, v52, v26 op_sel_hi:[0,1]\n"
    "v_pk_mul_f16 v93, v52, v27 op_sel_hi:[0,1]\n"
    "v_pk_mul_f16 v94, v52, v28 op_sel_hi:[0,1]\n"
    "v_pk_mul_f16 v95, v52, v29 op_sel_hi:[0,1]\n"
    "v_mfma_f32_32x32x16_f16 v[156:171], v[136:139], v[104:107], v[156:171]\n"
    "v_pk_mul_f16 v96, v59, v39 op_sel_hi:[0,1]\n"
    "v_pk_mul_f16 v97, v59, v40 op_sel_hi:[0,1]\n"
    "v_pk_mul_f16 v98, v59, v41 op_sel_hi:[0,1]\n"
    "v_pk_mul_f16 v99, v59, v42 op_sel_hi:[0,1]\n"
    "v_cvt_pk_f16_f32 v60, v10, v11\n"
    "v_mul_f32 v8, v210, v89\n"
    "v_mfma_f32_32x32x16_f16 v[172:187], v[124:127], v[92:95], 0\n"
    "ds_read_b128 v[136:139], v20 offset:48\n"
    "v_pk_mul_f16 v100, v52, v30 op_sel_hi:[0,1]\n"
    "v_pk_mul_f16 v101, v52, v31 op_sel_hi:[0,1]\n"
    "v_pk_mul_f16 v102, v52, v32 op_sel_hi:[0,1]\n"
    "v_pk_mul_f16 v103, v52, v33 op_sel_hi:[0,1]\n"
    "v_mfma_f32_32x32x16_f16 v[188:203], v[124:127], v[96:99], 0\n"
    "v_pk_mul_f16 v104, v59, v43 op_sel_hi:[0,1]\n"
    "v_pk_mul_f16 v105, v59, v44 op_sel_hi:[0,1]\n"
    "v_pk_mul_f16 v106, v59, v45 op_sel_hi:[0,1]\n"
    "v_pk_mul_f16 v107, v59, v46 op_sel_hi:[0,1]\n"
    "v_mul_f32 v9, v211, v89\n"
    "s_waitcnt lgkmcnt(1)\n"
    "v_mfma_f32_32x32x16_f16 v[172:187], v[128:131], v[100:103], v[172:187]\n"
    "ds_read_b128 v[124:127], v20 offset:64\n"
    "v_pk_mul_f16 v92, v52, v34 op_sel_hi:[0,1]\n"
    "v_pk_mul_f16 v93, v52, v35 op_sel_hi:[0,1]\n"
    "v_pk_mul_f16 v94, v52, v36 op_sel_hi:[0,1]\n"
    "v_pk_mul_f16 v95, v52, v37 op_sel_hi:[0,1]\n"
    "v_mfma_f32_32x32x16_f16 v[188:203], v[128:131], v[104:107], v[188:203]\n"
    "v_pk_mul_f16 v96, v59, v47 op_sel_hi:[0,1]\n"
    "v_pk_mul_f16 v97, v59, v48 op_sel_hi:[0,1]\n"
    "v_pk_mul_f16 v98, v59, v49 op_sel_hi:[0,1]\n"
    "v_pk_mul_f16 v99, v59, v50 op_sel_hi:[0,1]\n"
    "v_mul_f32 v10, v226, v90\n"
    "v_mul_f32 v11, v227, v90\n"
    "v_mfma_f32_32x32x16_f16 v[172:187], v[132:135], v[92:95], v[172:187]\n"
    "ds_read_b128 v[128:131], v20 offset:80\n"
    "v_pk_mul_f16 v100, v52, v38 op_sel_hi:[0,1]\n"
    "v_pk_mul_f16 v101, v52, v26 op_sel:[1,0]\n"
    "v_pk_mul_f16 v102, v52, v27 op_sel:[1,0]\n"
    "v_pk_mul_f16 v103, v52, v28 op_sel:[1,0]\n"
    "v_mfma_f32_32x32x16_f16 v[188:203], v[132:135], v[96:99], v[188:203]\n"
    "v_pk_mul_f16 v104, v59, v51 op_sel_hi:[0,1]\n"
    "v_pk_mul_f16 v105, v59, v39 op_sel:[1,0]\n"
    "v_pk_mul_f16 v106, v59, v40 op_sel:[1,0]\n"
    "v_pk_mul_f16 v107, v59, v41 op_sel:[1,0]\n"
    "v_cvt_pk_f16_f32 v54, v8, v9\n"
    "s_waitcnt lgkmcnt(1)\n"
    "v_mfma_f32_32x32x16_f16 v[172:187], v[136:139], v[100:103], v[172:187]\n"
    "ds_read_b128 v[132:135], v20 offset:96\n"
    "v_pk_mul_f16 v92, v52, v29 op_sel:[1,0]\n"
    "v_pk_mul_f16 v93, v52, v30 op_sel:[1,0]\n"
    "v_pk_mul_f16 v94, v52, v31 op_sel:[1,0]\n"
    "v_pk_mul_f16 v95, v52, v32 op_sel:[1,0]\n"
    "v_mfma_f32_32x32x16_f16 v[188:203], v[136:139], v[104:107], v[188:203]\n"
    "v_pk_mul_f16 v96, v59, v42 op_sel:[1,0]\n"
    "v_pk_mul_f16 v97, v59, v43 op_sel:[1,0]\n"
    "v_pk_mul_f16 v98, v59, v44 op_sel:[1,0]\n"
    "v_pk_mul_f16 v99, v59, v45 op_sel:[1,0]\n"
    "v_cvt_pk_f16_f32 v61, v10, v11\n"
    "v_mul_f32 v8, v206, v89\n"
    "v_mfma_f32_32x32x16_f16 v[172:187], v[124:127], v[92:95], v[172:187]\n"
    "ds_read_b128 v[136:139], v20 offset:112\n"
    "v_pk_mul_f16 v100, v52, v33 op_sel:[1,0]\n"
    "v_pk_mul_f16 v101, v52, v34 op_sel:[1,0]\n"
    "v_pk_mul_f16 v102, v52, v35 op_sel:[1,0]\n"
    "v_pk_mul_f16 v103, v52, v36 op_sel:[1,0]\n"
    "v_mfma_f32_32x32x16_f16 v[188:203], v[124:127], v[96:99], v[188:203]\n"
    "v_pk_mul_f16 v104, v59, v46 op_sel:[1,0]\n"
    "v_pk_mul_f16 v105, v59, v47 op_sel:[1,0]\n"
    "v_pk_mul_f16 v106, v59, v48 op_sel:[1,0]\n"
    "v_pk_mul_f16 v107, v59, v49 op_sel:[1,0]\n"
    "v_mul_f32 v9, v212, v89\n"
    "s_waitcnt lgkmcnt(1)\n"
    "v_mfma_f32_32x32x16_f16 v[172:187], v[128:131], v[100:103], v[172:187]\n"
    "ds_read_b128 v[124:127], v20 offset:128\n"
    "v_pk_mul_f16 v92, v52, v37 op_sel:[1,0]\n"
    "v_pk_mul_f16 v93, v52, v38 op_sel:[1,0]\n"
    "v_pk_mul_f16 v94, v53, v26 op_sel_hi:[0,1]\n"
    "v_pk_mul_f16 v95, v53, v27 op_sel_hi:[0,1]\n"
    "v_mfma_f32_32x32x16_f16 v[188:203], v[128:131], v[104:107], v[188:203]\n"
    "v_pk_mul_f16 v96, v59, v50 op_sel:[1,0]\n"
    "v_pk_mul_f16 v97, v59, v51 op_sel:[1,0]\n"
    "v_pk_mul_f16 v98, v60, v39 op_sel_hi:[0,1]\n"
    "v_pk_mul_f16 v99, v60, v40 op_sel_hi:[0,1]\n"
    "v_mul_f32 v10, v222, v90\n"
    "v_mul_f32 v11, v228, v90\n"
    "v_mfma_f32_32x32x16_f16 v[172:187], v[132:135], v[92:95], v[172:187]\n"
    "ds_read_b128 v[128:131], v20 offset:144\n"
    "v_pk_mul_f16 v100, v53, v28 op_sel_hi:[0,1]\n"
    "v_pk_mul_f16 v101, v53, v29 op_sel_hi:[0,1]\n"
    "v_pk_mul_f16 v102, v53, v30 op_sel_hi:[0,1]\n"
    "v_pk_mul_f16 v103, v53, v31 op_sel_hi:[0,1]\n"
    "v_mfma_f32_32x32x16_f16 v[188:203], v[132:135], v[96:99], v[188:203]\n"
    "v_pk_mul_f16 v104, v60, v41 op_sel_hi:[0,1]\n"
    "v_pk_mul_f16 v105, v60, v42 op_sel_hi:[0,1]\n"
    "v_pk_mul_f16 v106, v60, v43 op_sel_hi:[0,1]\n"
    "v_pk_mul_f16 v107, v60, v44 op_sel_hi:[0,1]\n"
    "v_cvt_pk_f16_f32 v55, v8, v9\n"
    "s_waitcnt lgkmcnt(1)\n"
    "v_mfma_f32_32x32x16_f16 v[172:187], v[136:139], v[100:103], v[172:187]\n"
    "ds_read_b128 v[132:135], v20 offset:160\n"
    "v_pk_mul_f16 v92, v53, v32 op_sel_hi:[0,1]\n"
    "v_pk_mul_f16 v93, v53, v33 op_sel_hi:[0,1]\n"
    "v_pk_mul_f16 v94, v53, v34 op_sel_hi:[0,1]\n"
    "v_pk_mul_f16 v95, v53, v35 op_sel_hi:[0,1]\n"
    "v_mfma_f32_32x32x16_f16 v[188:203], v[136:139], v[104:107], v[188:203]\n"
    "v_pk_mul_f16 v96, v60, v45 op_sel_hi:[0,1]\n"
    "v_pk_mul_f16 v97, v60, v46 op_sel_hi:[0,1]\n"
    "v_pk_mul_f16 v98, v60, v47 op_sel_hi:[0,1]\n"
    "v_pk_mul_f16 v99, v60, v48 op_sel_hi:[0,1]\n"
    "v_cvt_pk_f16_f32 v62, v10, v11\n"
    "v_mul_f32 v8, v213, v89\n"
    "v_mfma_f32_32x32x16_f16 v[172:187], v[124:127], v[92:95], v[172:187]\n"
    "ds_read_b128 v[136:139], v20 offset:176\n"
    "v_pk_mul_f16 v100, v53, v36 op_sel_hi:[0,1]\n"
    "v_pk_mul_f16 v101, v53, v37 op_sel_hi:[0,1]\n"
    "v_pk_mul_f16 v102, v53, v38 op_sel_hi:[0,1]\n"
    "v_pk_mul_f16 v103, v53, v26 op_sel:[1,0]\n"
    "v_mfma_f32_32x32x16_f16 v[188:203], v[124:127], v[96:99], v[188:203]\n"
    "v_pk_mul_f16 v104, v60, v49 op_sel_hi:[0,1]\n"
    "v_pk_mul_f16 v105, v60, v50 op_sel_hi:[0,1]\n"
    "v_pk_mul_f16 v106, v60, v51 op_sel_hi:[0,1]\n"
    "v_pk_mul_f16 v107, v60, v39 op_sel:[1,0]\n"
    "v_mul_f32 v9, v207, v89\n"
    "s_waitcnt lgkmcnt(1)\n"
    "v_mfma_f32_32x32x16_f16 v[172:187], v[128:131], v[100:103], v[172:187]\n"
    "ds_read_b128 v[124:127], v20 offset:192\n"
    "v_pk_mul_f16 v92, v53, v27 op_sel:[1,0]\n"
    "v_pk_mul_f16 v93, v53, v28 op_sel:[1,0]\n"
    "v_pk_mul_f16 v94, v53, v29 op_sel:[1,0]\n"
    "v_pk_mul_f16 v95, v53, v30 op_sel:[1,0]\n"
    "v_mfma_f32_32x32x16_f16 v[188:203], v[128:131], v[104:107], v[188:203]\n"
    "v_pk_mul_f16 v96, v60, v40 op_sel:[1,0]\n"
    "v_pk_mul_f16 v97, v60, v41 op_sel:[1,0]\n"
    "v_pk_mul_f16 v98, v60, v42 op_sel:[1,0]\n"
    "v_pk_mul_f16 v99, v60, v43 op_sel:[1,0]\n"
    "v_mul_f32 v10, v229, v90\n"
    "v_mul_f32 v11, v223, v90\n"
    "v_mfma_f32_32x32x16_f16 v[172:187], v[132:135], v[92:95], v[172:187]\n"
    "ds_read_b128 v[128:131], v20 offset:208\n"
    "v_pk_mul_f16 v100, v53, v31 op_sel:[1,0]\n"
    "v_pk_mul_f16 v101, v53, v32 op_sel:[1,0]\n"
    "v_pk_mul_f16 v102, v53, v33 op_sel:[1,0]\n"
    "v_pk_mul_f16 v103, v53, v34 op_sel:[1,0]\n"
    "v_mfma_f32_32x32x16_f16 v[188:203], v[132:135], v[96:99], v[188:203]\n"
    "v_pk_mul_f16 v104, v60, v44 op_sel:[1,0]\n"
    "v_pk_mul_f16 v105, v60, v45 op_sel:[1,0]\n"
    "v_pk_mul_f16 v106, v60, v46 op_sel:[1,0]\n"
    "v_pk_mul_f16 v107, v60, v47 op_sel:[1,0]\n"
    "v_cvt_pk_f16_f32 v56, v8, v9\n"
    "s_waitcnt lgkmcnt(1)\n"
    "v_mfma_f32_32x32x16_f16 v[172:187], v[136:139], v[100:103], v[172:187]\n"
    "ds_read_b128 v[132:135], v20 offset:224\n"
    "v_pk_mul_f16 v92, v53, v35 op_sel:[1,0]\n"
    "v_pk_mul_f16 v93, v53, v36 op_sel:[1,0]\n"
    "v_pk_mul_f16 v94, v53, v37 op_sel:[1,0]\n"
    "v_pk_mul_f16 v95, v53, v38 op_sel:[1,0]\n"
    "v_mfma_f32_32x32x16_f16 v[188:203], v[136:139], v[104:107], v[188:203]\n"
    "v_pk_mul_f16 v96, v60, v48 op_sel:[1,0]\n"
    "v_pk_mul_f16 v97, v60, v49 op_sel:[1,0]\n"
    "v_pk_mul_f16 v98, v60, v50 op_sel:[1,0]\n"
    "v_pk_mul_f16 v99, v60, v51 op_sel:[1,0]\n"
    "v_cvt_pk_f16_f32 v63, v10, v11\n"
    "v_mul_f32 v8, v214, v89\n"
    "v_mfma_f32_32x32x16_f16 v[172:187], v[124:127], v[92:95], v[172:187]\n"
    "ds_read_b128 v[136:139], v20 offset:240\n"
    "v_pk_mul_f16 v100, v54, v26 op_sel_hi:[0,1]\n"
    "v_pk_mul_f16 v101, v54, v27 op_sel_hi:[0,1]\n"
    "v_pk_mul_f16 v102, v54, v28 op_sel_hi:[0,1]\n"
    "v_pk_mul_f16 v103, v54, v29 op_sel_hi:[0,1]\n"
    "v_mfma_f32_32x32x16_f16 v[188:203], v[124:127], v[96:99], v[188:203]\n"
    "v_pk_mul_f16 v104, v61, v39 op_sel_hi:[0,1]\n"
    "v_pk_mul_f16 v105, v61, v40 op_sel_hi:[0,1]\n"
    "v_pk_mul_f16 v106, v61, v41 op_sel_hi:[0,1]\n"
    "v_pk_mul_f16 v107, v61, v42 op_sel_hi:[0,1]\n"
    "v_mul_f32 v9, v215, v89\n"
    "s_waitcnt lgkmcnt(1)\n"
    "v_mfma_f32_32x32x16_f16 v[172:187], v[128:131], v[100:103], v[172:187]\n"
    "ds_read_b128 v[124:127], v20 offset:256\n"
    "v_pk_mul_f16 v92, v54, v30 op_sel_hi:[0,1]\n"
    "v_pk_mul_f16 v93, v54, v31 op_sel_hi:[0,1]\n"
    "v_pk_mul_f16 v94, v54, v32 op_sel_hi:[0,1]\n"
    "v_pk_mul_f16 v95, v54, v33 op_sel_hi:[0,1]\n"
    "v_mfma_f32_32x32x16_f16 v[188:203], v[128:131], v[104:107], v[188:203]\n"
    "v_pk_mul_f16 v96, v61, v43 op_sel_hi:[0,1]\n"
    "v_pk_mul_f16 v97, v61, v44 op_sel_hi:[0,1]\n"
    "v_pk_mul_f16 v98, v61, v45 op_sel_hi:[0,1]\n"
    "v_pk_mul_f16 v99, v61, v46 op_sel_hi:[0,1]\n"
    "v_mul_f32 v10, v230, v90\n"
    "v_mul_f32 v11, v231, v90\n"
    "v_mfma_f32_32x32x16_f16 v[172:187], v[132:135], v[92:95], v[172:187]\n"
    "ds_read_b128 v[128:131], v20 offset:272\n"
    "v_pk_mul_f16 v100, v54, v34 op_sel_hi:[0,1]\n"
    "v_pk_mul_f16 v101, v54, v35 op_sel_hi:[0,1]\n"
    "v_pk_mul_f16 v102, v54, v36 op_sel_hi:[0,1]\n"
    "v_pk_mul_f16 v103, v54, v37 op_sel_hi:[0,1]\n"
    "v_mfma_f32_32x32x16_f16 v[188:203], v[132:135], v[96:99], v[188:203]\n"
    "v_pk_mul_f16 v104, v61, v47 op_sel_hi:[0,1]\n"
    "v_pk_mul_f16 v105, v61, v48 op_sel_hi:[0,1]\n"
    "v_pk_mul_f16 v106, v61, v49 op_sel_hi:[0,1]\n"
    "v_pk_mul_f16 v107, v61, v50 op_sel_hi:[0,1]\n"
    "v_cvt_pk_f16_f32 v57, v8, v9\n"
    "s_waitcnt lgkmcnt(1)\n"
    "v_mfma_f32_32x32x16_f16 v[172:187], v[136:139], v[100:103], v[172:187]\n"
    "ds_read_b128 v[132:135], v20 offset:288\n"
    "v_pk_mul_f16 v92, v54, v38 op_sel_hi:[0,1]\n"
    "v_pk_mul_f16 v93, v54, v26 op_sel:[1,0]\n"
    "v_pk_mul_f16 v94, v54, v27 op_sel:[1,0]\n"
    "v_pk_mul_f16 v95, v54, v28 op_sel:[1,0]\n"
    "v_mfma_f32_32x32x16_f16 v[188:203], v[136:139], v[104:107], v[188:203]\n"
    "v_pk_mul_f16 v96, v61, v51 op_sel_hi:[0,1]\n"
    "v_pk_mul_f16 v97, v61, v39 op_sel:[1,0]\n"
    "v_pk_mul_f16 v98, v61, v40 op_sel:[1,0]\n"
    "v_pk_mul_f16 v99, v61, v41 op_sel:[1,0]\n"
    "v_cvt_pk_f16_f32 v64, v10, v11\n"
    "v_mul_f32 v8, v216, v89\n"
    "v_mfma_f32_32x32x16_f16 v[172:187], v[124:127], v[92:95], v[172:187]\n"
    "ds_read_b128 v[136:139], v20 offset:304\n"
    "v_pk_mul_f16 v100, v54, v29 op_sel:[1,0]\n"
    "v_pk_mul_f16 v101, v54, v30 op_sel:[1,0]\n"
    "v_pk_mul_f16 v102, v54, v31 op_sel:[1,0]\n"
    "v_pk_mul_f16 v103, v54, v32 op_sel:[1,0]\n"
    "v_mfma_f32_32x32x16_f16 v[188:203], v[124:127], v[96:99], v[188:203]\n"
    "v_pk_mul_f16 v104, v61, v42 op_sel:[1,0]\n"
    "v_pk_mul_f16 v105, v61, v43 op_sel:[1,0]\n"
    "v_pk_mul_f16 v106, v61, v44 op_sel:[1,0]\n"
    "v_pk_mul_f16 v107, v61, v45 op_sel:[1,0]\n"
    "v_mul_f32 v10, v232, v90\n"
    "s_waitcnt lgkmcnt(1)\n"
    "v_mfma_f32_32x32x16_f16 v[172:187], v[128:131], v[100:103], v[172:187]\n"
    "ds_read_b128 v[124:127], v20 offset:320\n"
    "v_pk_mul_f16 v92, v54, v33 op_sel:[1,0]\n"
    "v_pk_mul_f16 v93, v54, v34 op_sel:[1,0]\n"
    "v_pk_mul_f16 v94, v54, v35 op_sel:[1,0]\n"
    "v_pk_mul_f16 v95, v54, v36 op_sel:[1,0]\n"
    "v_mfma_f32_32x32x16_f16 v[188:203], v[128:131], v[104:107], v[188:203]\n"
    "v_pk_mul_f16 v96, v61, v46 op_sel:[1,0]\n"
    "v_pk_mul_f16 v97, v61, v47 op_sel:[1,0]\n"
    "v_pk_mul_f16 v98, v61, v48 op_sel:[1,0]\n"
    "v_pk_mul_f16 v99, v61, v49 op_sel:[1,0]\n"
    "v_cvt_pk_f16_f32 v58, v8, v8\n"
    "v_cvt_pk_f16_f32 v65, v10, v10\n"
    "v_mfma_f32_32x32x16_f16 v[172:187], v[132:135], v[92:95], v[172:187]\n"
    "ds_read_b128 v[128:131], v20 offset:336\n"
    "v_pk_mul_f16 v100, v54, v37 op_sel:[1,0]\n"
    "v_pk_mul_f16 v101, v54, v38 op_sel:[1,0]\n"
    "v_pk_mul_f16 v102, v55, v26 op_sel_hi:[0,1]\n"
    "v_pk_mul_f16 v103, v55, v27 op_sel_hi:[0,1]\n"
    "v_mfma_f32_32x32x16_f16 v[188:203], v[132:135], v[96:99], v[188:203]\n"
    "v_pk_mul_f16 v104, v61, v50 op_sel:[1,0]\n"
    "v_pk_mul_f16 v105, v61, v51 op_sel:[1,0]\n"
    "LM3_%=:\n"
    "s_cbranch_execz LMR_%=\n"
    "v_pk_mul_f16 v106, v62, v39 op_sel_hi:[0,1]\n"
    "v_pk_mul_f16 v107, v62, v40 op_sel_hi:[0,1]\n"
    "v_mul_f32 v84, v84, v7\n"
    "s_waitcnt lgkmcnt(1)\n"
    "v_mfma_f32_32x32x16_f16 v[172:187], v[136:139], v[100:103], v[172:187]\n"
    "ds_read_b128 v[132:135], v20 offset:352\n"
    "v_pk_mul_f16 v92, v55, v28 op_sel_hi:[0,1]\n"
    "v_pk_mul_f16 v93, v55, v29 op_sel_hi:[0,1]\n"
    "v_pk_mul_f16 v94, v55, v30 op_sel_hi:[0,1]\n"
    "v_pk_mul_f16 v95, v55, v31 op_sel_hi:[0,1]\n"
    "v_mfma_f32_32x32x16_f16 v[188:203], v[136:139], v[104:107], v[188:203]\n"
    "v_pk_mul_f16 v96, v62, v41 op_sel_hi:[0,1]\n"
    "v_pk_mul_f16 v97, v62, v42 op_sel_hi:[0,1]\n"
    "v_pk_mul_f16 v98, v62, v43 op_sel_hi:[0,1]\n"
    "v_pk_mul_f16 v99, v62, v44 op_sel_hi:[0,1]\n"
    "v_mul_f32 v84, 0x39800000, v84\n"
    "v_mul_f32 v12, v236, v140\n"
    "v_mfma_f32_32x32x16_f16 v[172:187], v[124:127], v[92:95], v[172:187]\n"
    "ds_read_b128 v[136:139], v20 offset:368\n"
    "v_pk_mul_f16 v100, v55, v32 op_sel_hi:[0,1]\n"
    "v_pk_mul_f16 v101, v55, v33 op_sel_hi:[0,1]\n"
    "v_pk_mul_f16 v102, v55, v34 op_sel_hi:[0,1]\n"
    "v_pk_mul_f16 v103, v55, v35 op_sel_hi:[0,1]\n"
    "v_mfma_f32_32x32x16_f16 v[188:203], v[124:127], v[96:99], v[188:203]\n"
    "v_pk_mul_f16 v104, v62, v45 op_sel_hi:[0,1]\n"
    "v_pk_mul_f16 v105, v62, v46 op_sel_hi:[0,1]\n"
    "v_pk_mul_f16 v106, v62, v47 op_sel_hi:[0,1]\n"
    "v_pk_mul_f16 v107, v62, v48 op_sel_hi:[0,1]\n"
    "v_mul_f32 v13, v236, v156\n"
    "s_waitcnt lgkmcnt(1)\n"
    "v_mfma_f32_32x32x16_f16 v[172:187], v[128:131], v[100:103], v[172:187]\n"
    "ds_read_b128 v[124:127], v20 offset:384\n"
    "v_pk_mul_f16 v92, v55, v36 op_sel_hi:[0,1]\n"
    "v_pk_mul_f16 v93, v55, v37 op_sel_hi:[0,1]\n"
    "v_pk_mul_f16 v94, v55, v38 op_sel_hi:[0,1]\n"
    "v_pk_mul_f16 v95, v55, v26 op_sel:[1,0]\n"
    "v_mfma_f32_32x32x16_f16 v[188:203], v[128:131], v[104:107], v[188:203]\n"
    "v_pk_mul_f16 v96, v62, v49 op_sel_hi:[0,1]\n"
    "v_pk_mul_f16 v97, v62, v50 op_sel_hi:[0,1]\n"
    "v_pk_mul_f16 v98, v62, v51 op_sel_hi:[0,1]\n"
    "v_pk_mul_f16 v99, v62, v39 op_sel:[1,0]\n"
    "v_fmac_f32 v12, v237, v141\n"
    "v_fmac_f32 v13, v237, v157\n"
    "v_mfma_f32_32x32x16_f16 v[172:187], v[132:135], v[92:95], v[172:187]\n"
    "ds_read_b128 v[128:131], v20 offset:400\n"
    "v_pk_mul_f16 v100, v55, v27 op_sel:[1,0]\n"
    "v_pk_mul_f16 v101, v55, v28 op_sel:[1,0]\n"
    "v_pk_mul_f16 v102, v55, v29 op_sel:[1,0]\n"
    "v_pk_mul_f16 v103, v55, v30 op_sel:[1,0]\n"
    "v_mfma_f32_32x32x16_f16 v[188:203], v[132:135], v[96:99], v[188:203]\n"
    "v_pk_mul_f16 v104, v62, v40 op_sel:[1,0]\n"
    "v_pk_mul_f16 v105, v62, v41 op_sel:[1,0]\n"
    "v_pk_mul_f16 v106, v62, v42 op_sel:[1,0]\n"
    "v_pk_mul_f16 v107, v62, v43 op_sel:[1,0]\n"
    "v_fmac_f32 v12, v238, v142\n"
    "s_waitcnt lgkmcnt(1)\n"
    "v_mfma_f32_32x32x16_f16 v[172:187], v[136:139], v[100:103], v[172:187]\n"
    "ds_read_b128 v[132:135], v20 offset:416\n"
    "v_pk_mul_f16 v92, v55, v31 op_sel:[1,0]\n"
    "v_pk_mul_f16 v93, v55, v32 op_sel:[1,0]\n"
    "v_pk_mul_f16 v94, v55, v33 op_sel:[1,0]\n"
    "v_pk_mul_f16 v95, v55, v34 op_sel:[1,0]\n"
    "v_mfma_f32_32x32x16_f16 v[188:203], v[136:139], v[104:107], v[188:203]\n"
    "v_pk_mul_f16 v96, v62, v44 op_sel:[1,0]\n"
    "v_pk_mul_f16 v97, v62, v45 op_sel:[1,0]\n"
    "v_pk_mul_f16 v98, v62, v46 op_sel:[1,0]\n"
    "v_pk_mul_f16 v99, v62, v47 op_sel:[1,0]\n"
    "v_fmac_f32 v13, v238, v158\n"
    "v_fmac_f32 v12, v239, v143\n"
    "v_mfma_f32_32x32x16_f16 v[172:187], v[124:127], v[92:95], v[172:187]\n"
    "ds_read_b128 v[136:139], v20 offset:432\n"
    "v_pk_mul_f16 v100, v55, v35 op_sel:[1,0]\n"
    "v_pk_mul_f16 v101, v55, v36 op_sel:[1,0]\n"
    "v_pk_mul_f16 v102, v55, v37 op_sel:[1,0]\n"
    "v_pk_mul_f16 v103, v55, v38 op_sel:[1,0]\n"
    "v_mfma_f32_32x32x16_f16 v[188:203], v[124:127], v[96:99], v[188:203]\n"
    "v_pk_mul_f16 v104, v62, v48 op_sel:[1,0]\n"
    "v_pk_mul_f16 v105, v62, v49 op_sel:[1,0]\n"
    "v_pk_mul_f16 v106, v62, v50 op_sel:[1,0]\n"
    "v_pk_mul_f16 v107, v62, v51 op_sel:[1,0]\n"
    "v_fmac_f32 v13, v239, v159\n"
    "s_waitcnt lgkmcnt(1)\n"
    "v_mfma_f32_32x32x16_f16 v[172:187], v[128:131], v[100:103], v[172:187]\n"
    "ds_read_b128 v[124:127], v20 offset:448\n"
    "v_pk_mul_f16 v92, v56, v26 op_sel_hi:[0,1]\n"
    "v_pk_mul_f16 v93, v56, v27 op_sel_hi:[0,1]\n"
    "v_pk_mul_f16 v94, v56, v28 op_sel_hi:[0,1]\n"
    "v_pk_mul_f16 v95, v56, v29 op_sel_hi:[0,1]\n"
    "v_mfma_f32_32x32x16_f16 v[188:203], v[128:131], v[104:107], v[188:203]\n"
    "v_pk_mul_f16 v96, v63, v39 op_sel_hi:[0,1]\n"
    "v_pk_mul_f16 v97, v63, v40 op_sel_hi:[0,1]\n"
    "v_pk_mul_f16 v98, v63, v41 op_sel_hi:[0,1]\n"
    "v_pk_mul_f16 v99, v63, v42 op_sel_hi:[0,1]\n"
    "v_fmac_f32 v12, v240, v144\n"
    "v_fmac_f32 v13, v240, v160\n"
    "v_mfma_f32_32x32x16_f16 v[172:187], v[132:135], v[92:95], v[172:187]\n"
    "ds_read_b128 v[128:131], v20 offset:464\n"
    "v_pk_mul_f16 v100, v56, v30 op_sel_hi:[0,1]\n"
    "v_pk_mul_f16 v101, v56, v31 op_sel_hi:[0,1]\n"
    "v_pk_mul_f16 v102, v56, v32 op_sel_hi:[0,1]\n"
    "v_pk_mul_f16 v103, v56, v33 op_sel_hi:[0,1]\n"
    "v_mfma_f32_32x32x16_f16 v[188:203], v[132:135], v[96:99], v[188:203]\n"
    "v_pk_mul_f16 v104, v63, v43 op_sel_hi:[0,1]\n"
    "v_pk_mul_f16 v105, v63, v44 op_sel_hi:[0,1]\n"
    "v_pk_mul_f16 v106, v63, v45 op_sel_hi:[0,1]\n"
    "v_pk_mul_f16 v107, v63, v46 op_sel_hi:[0,1]\n"
    "v_fmac_f32 v12, v241, v145\n"
    "s_waitcnt lgkmcnt(1)\n"
    "v_mfma_f32_32x32x16_f16 v[172:187], v[136:139], v[100:103], v[172:187]\n"
    "ds_read_b128 v[132:135], v20 offset:480\n"
    "v_pk_mul_f16 v92, v56, v34 op_sel_hi:[0,1]\n"
    "v_pk_mul_f16 v93, v56, v35 op_sel_hi:[0,1]\n"
    "v_pk_mul_f16 v94, v56, v36 op_sel_hi:[0,1]\n"
    "v_pk_mul_f16 v95, v56, v37 op_sel_hi:[0,1]\n"
    "v_mfma_f32_32x32x16_f16 v[188:203], v[136:139], v[104:107], v[188:203]\n"
    "v_pk_mul_f16 v96, v63, v47 op_sel_hi:[0,1]\n"
    "v_pk_mul_f16 v97, v63, v48 op_sel_hi:[0,1]\n"
    "v_pk_mul_f16 v98, v63, v49 op_sel_hi:[0,1]\n"
    "v_pk_mul_f16 v99, v63, v50 op_sel_hi:[0,1]\n"
    "v_fmac_f32 v13, v241, v161\n"
    "v_fmac_f32 v12, v242, v146\n"
    "v_mfma_f32_32x32x16_f16 v[172:187], v[124:127], v[92:95], v[172:187]\n"
    "ds_read_b128 v[136:139], v20 offset:496\n"
    "v_pk_mul_f16 v100, v56, v38 op_sel_hi:[0,1]\n"
    "v_pk_mul_f16 v101, v56, v26 op_sel:[1,0]\n"
    "v_pk_mul_f16 v102, v56, v27 op_sel:[1,0]\n"
    "v_pk_mul_f16 v103, v56, v28 op_sel:[1,0]\n"
    "v_mfma_f32_32x32x16_f16 v[188:203], v[124:127], v[96:99], v[188:203]\n"
    "v_pk_mul_f16 v104, v63, v51 op_sel_hi:[0,1]\n"
    "v_pk_mul_f16 v105, v63, v39 op_sel:[1,0]\n"
    "v_pk_mul_f16 v106, v63, v40 op_sel:[1,0]\n"
    "v_pk_mul_f16 v107, v63, v41 op_sel:[1,0]\n"
    "v_fmac_f32 v13, v242, v162\n"
    "s_waitcnt lgkmcnt(1)\n"
    "v_mfma_f32_32x32x16_f16 v[172:187], v[128:131], v[100:103], v[172:187]\n"
    "ds_read_b128 v[124:127], v20 offset:512\n"
    "v_pk_mul_f16 v92, v56, v29 op_sel:[1,0]\n"
    "v_pk_mul_f16 v93, v56, v30 op_sel:[1,0]\n"
    "v_pk_mul_f16 v94, v56, v31 op_sel:[1,0]\n"
    "v_pk_mul_f16 v95, v56, v32 op_sel:[1,0]\n"
    "v_mfma_f32_32x32x16_f16 v[188:203], v[128:131], v[104:107], v[188:203]\n"
    "v_pk_mul_f16 v96, v63, v42 op_sel:[1,0]\n"
    "v_pk_mul_f16 v97, v63, v43 op_sel:[1,0]\n"
    "v_pk_mul_f16 v98, v63, v44 op_sel:[1,0]\n"
    "v_pk_mul_f16 v99, v63, v45 op_sel:[1,0]\n"
    "v_fmac_f32 v12, v243, v147\n"
    "v_fmac_f32 v13, v243, v163\n"
    "v_mfma_f32_32x32x16_f16 v[172:187], v[132:135], v[92:95], v[172:187]\n"
    "ds_read_b128 v[128:131], v20 offset:528\n"
    "v_pk_mul_f16 v100, v56, v33 op_sel:[1,0]\n"
    "v_pk_mul_f16 v101, v56, v34 op_sel:[1,0]\n"
    "v_pk_mul_f16 v102, v56, v35 op_sel:[1,0]\n"
    "v_pk_mul_f16 v103, v56, v36 op_sel:[1,0]\n"
    "v_mfma_f32_32x32x16_f16 v[188:203], v[132:135], v[96:99], v[188:203]\n"
    "v_pk_mul_f16 v104, v63, v46 op_sel:[1,0]\n"
    "v_pk_mul_f16 v105, v63, v47 op_sel:[1,0]\n"
    "v_pk_mul_f16 v106, v63, v48 op_sel:[1,0]\n"
    "v_pk_mul_f16 v107, v63, v49 op_sel:[1,0]\n"
    "v_fmac_f32 v12, v244, v148\n"
    "s_waitcnt lgkmcnt(1)\n"
    "v_mfma_f32_32x32x16_f16 v[172:187], v[136:139], v[100:103], v[172:187]\n"
    "ds_read_b128 v[132:135], v20 offset:544\n"
    "v_pk_mul_f16 v92, v56, v37 op_sel:[1,0]\n"
    "v_pk_mul_f16 v93, v56, v38 op_sel:[1,0]\n"
    "v_pk_mul_f16 v94, v57, v26 op_sel_hi:[0,1]\n"
    "v_pk_mul_f16 v95, v57, v27 op_sel_hi:[0,1]\n"
    "v_mfma_f32_32x32x16_f16 v[188:203], v[136:139], v[104:107], v[188:203]\n"
    "v_pk_mul_f16 v96, v63, v50 op_sel:[1,0]\n"
    "v_pk_mul_f16 v97, v63, v51 op_sel:[1,0]\n"
    "v_pk_mul_f16 v98, v64, v39 op_sel_hi:[0,1]\n"
    "v_pk_mul_f16 v99, v64, v40 op_sel_hi:[0,1]\n"
    "v_fmac_f32 v13, v244, v164\n"
    "v_fmac_f32 v12, v245, v149\n"
    "v_mfma_f32_32x32x16_f16 v[172:187], v[124:127], v[92:95], v[172:187]\n"
    "ds_read_b128 v[136:139], v20 offset:560\n"
    "v_pk_mul_f16 v100, v57, v28 op_sel_hi:[0,1]\n"
    "v_pk_mul_f16 v101, v57, v29 op_sel_hi:[0,1]\n"
    "v_pk_mul_f16 v102, v57, v30 op_sel_hi:[0,1]\n"
    "v_pk_mul_f16 v103, v57, v31 op_sel_hi:[0,1]\n"
    "v_mfma_f32_32x32x16_f16 v[188:203], v[124:127], v[96:99], v[188:203]\n"
    "v_pk_mul_f16 v104, v64, v41 op_sel_hi:[0,1]\n"
    "v_pk_mul_f16 v105, v64, v42 op_sel_hi:[0,1]\n"
    "v_pk_mul_f16 v106, v64, v43 op_sel_hi:[0,1]\n"
    "v_pk_mul_f16 v107, v64, v44 op_sel_hi:[0,1]\n"
    "v_fmac_f32 v13, v245, v165\n"
    "s_waitcnt lgkmcnt(1)\n"
    "v_mfma_f32_32x32x16_f16 v[172:187], v[128:131], v[100:103], v[172:187]\n"
    "ds_read_b128 v[124:127], v20 offset:576\n"
    "v_pk_mul_f16 v92, v57, v32 op_sel_hi:[0,1]\n"
    "v_pk_mul_f16 v93, v57, v33 op_sel_hi:[0,1]\n"
    "v_pk_mul_f16 v94, v57, v34 op_sel_hi:[0,1]\n"
    "v_pk_mul_f16 v95, v57, v35 op_sel_hi:[0,1]\n"
    "v_mfma_f32_32x32x16_f16 v[188:203], v[128:131], v[104:107], v[188:203]\n"
    "v_pk_mul_f16 v96, v64, v45 op_sel_hi:[0,1]\n"
    "v_pk_mul_f16 v97, v64, v46 op_sel_hi:[0,1]\n"
    "v_pk_mul_f16 v98, v64, v47 op_sel_hi:[0,1]\n"
    "v_pk_mul_f16 v99, v64, v48 op_sel_hi:[0,1]\n"
    "v_fmac_f32 v12, v246, v150\n"
    "v_fmac_f32 v13, v246, v166\n"
    "v_mfma_f32_32x32x16_f16 v[172:187], v[132:135], v[92:95], v[172:187]\n"
    "ds_read_b128 v[128:131], v20 offset:592\n"
    "v_pk_mul_f16 v100, v57, v36 op_sel_hi:[0,1]\n"
    "v_pk_mul_f16 v101, v57, v37 op_sel_hi:[0,1]\n"
    "v_pk_mul_f16 v102, v57, v38 op_sel_hi:[0,1]\n"
    "v_pk_mul_f16 v103, v57, v26 op_sel:[1,0]\n"
    "v_mfma_f32_32x32x16_f16 v[188:203], v[132:135], v[96:99], v[188:203]\n"
    "v_pk_mul_f16 v104, v64, v49 op_sel_hi:[0,1]\n"
    "v_pk_mul_f16 v105, v64, v50 op_sel_hi:[0,1]\n"
    "v_pk_mul_f16 v106, v64, v51 op_sel_hi:[0,1]\n"
    "v_pk_mul_f16 v107, v64, v39 op_sel:[1,0]\n"
    "v_fmac_f32 v12, v247, v151\n"
    "s_waitcnt lgkmcnt(1)\n"
    "v_mfma_f32_32x32x16_f16 v[172:187], v[136:139], v[100:103], v[172:187]\n"
    "ds_read_b128 v[132:135], v20 offset:608\n"
    "v_pk_mul_f16 v92, v57, v27 op_sel:[1,0]\n"
    "v_pk_mul_f16 v93, v57, v28 op_sel:[1,0]\n"
    "v_pk_mul_f16 v94, v57, v29 op_sel:[1,0]\n"
    "v_pk_mul_f16 v95, v57, v30 op_sel:[1,0]\n"
    "v_mfma_f32_32x32x16_f16 v[188:203], v[136:139], v[104:107], v[188:203]\n"
    "v_pk_mul_f16 v96, v64, v40 op_sel:[1,0]\n"
    "v_pk_mul_f16 v97, v64, v41 op_sel:[1,0]\n"
    "v_pk_mul_f16 v98, v64, v42 op_sel:[1,0]\n"
    "v_pk_mul_f16 v99, v64, v43 op_sel:[1,0]\n"
    "v_fmac_f32 v13, v247, v167\n"
    "v_fmac_f32 v12, v248, v152\n"
    "v_mfma_f32_32x32x16_f16 v[172:187], v[124:127], v[92:95], v[172:187]\n"
    "ds_read_b128 v[136:139], v20 offset:624\n"
    "v_pk_mul_f16 v100, v57, v31 op_sel:[1,0]\n"
    "v_pk_mul_f16 v101, v57, v32 op_sel:[1,0]\n"
    "v_pk_mul_f16 v102, v57, v33 op_sel:[1,0]\n"
    "v_pk_mul_f16 v103, v57, v34 op_sel:[1,0]\n"
    "v_mfma_f32_32x32x16_f16 v[188:203], v[124:127], v[96:99], v[188:203]\n"
    "v_pk_mul_f16 v104, v64, v44 op_sel:[1,0]\n"
    "v_pk_mul_f16 v105, v64, v45 op_sel:[1,0]\n"
    "v_pk_mul_f16 v106, v64, v46 op_sel:[1,0]\n"
    "v_pk_mul_f16 v107, v64, v47 op_sel:[1,0]\n"
    "v_fmac_f32 v13, v248, v168\n"
    "s_waitcnt lgkmcnt(1)\n"
    "v_mfma_f32_32x32x16_f16 v[172:187], v[128:131], v[100:103], v[172:187]\n"
    "ds_read_b128 v[124:127], v20 offset:640\n"
    "v_pk_mul_f16 v92, v57, v35 op_sel:[1,0]\n"
    "v_pk_mul_f16 v93, v57, v36 op_sel:[1,0]\n"
    "v_pk_mul_f16 v94, v57, v37 op_sel:[1,0]\n"
    "v_pk_mul_f16 v95, v57, v38 op_sel:[1,0]\n"
    "v_mfma_f32_32x32x16_f16 v[188:203], v[128:131], v[104:107], v[188:203]\n"
    "v_pk_mul_f16 v96, v64, v48 op_sel:[1,0]\n"
    "v_pk_mul_f16 v97, v64, v49 op_sel:[1,0]\n"
    "v_pk_mul_f16 v98, v64, v50 op_sel:[1,0]\n"
    "v_pk_mul_f16 v99, v64, v51 op_sel:[1,0]\n"
    "v_permlane32_swap_b32 v12, v13\n"
    "v_add_f32 v86, v12, v13\n"
    "ds_read_b32 v19, v17 offset:40\n"
    "v_mfma_f32_32x32x16_f16 v[172:187], v[132:135], v[92:95], v[172:187]\n"
    "ds_read_b128 v[128:131], v20 offset:656\n"
    "v_pk_mul_f16 v100, v58, v26 op_sel_hi:[0,1]\n"
    "v_pk_mul_f16 v101, v58, v27 op_sel_hi:[0,1]\n"
    "v_pk_mul_f16 v102, v58, v28 op_sel_hi:[0,1]\n"
    "v_pk_mul_f16 v103, v58, v29 op_sel_hi:[0,1]\n"
    "v_mfma_f32_32x32x16_f16 v[188:203], v[132:135], v[96:99], v[188:203]\n"
    "v_pk_mul_f16 v104, v65, v39 op_sel_hi:[0,1]\n"
    "v_pk_mul_f16 v105, v65, v40 op_sel_hi:[0,1]\n"
    "v_pk_mul_f16 v106, v65, v41 op_sel_hi:[0,1]\n"
    "v_pk_mul_f16 v107, v65, v42 op_sel_hi:[0,1]\n"
    "v_fmac_f32 v85, v86, v83\n"
    "s_waitcnt lgkmcnt(2)\n"
    "v_mfma_f32_32x32x16_f16 v[172:187], v[136:139], v[100:103], v[172:187]\n"
    "ds_read_b128 v[132:135], v20 offset:672\n"
    "v_pk_mul_f16 v92, v58, v30 op_sel_hi:[0,1]\n"
    "v_pk_mul_f16 v93, v58, v31 op_sel_hi:[0,1]\n"
    "v_pk_mul_f16 v94, v58, v32 op_sel_hi:[0,1]\n"
    "v_pk_mul_f16 v95, v58, v33 op_sel_hi:[0,1]\n"
    "v_mfma_f32_32x32x16_f16 v[188:203], v[136:139], v[104:107], v[188:203]\n"
    "v_pk_mul_f16 v96, v65, v43 op_sel_hi:[0,1]\n"
    "v_pk_mul_f16 v97, v65, v44 op_sel_hi:[0,1]\n"
    "v_pk_mul_f16 v98, v65, v45 op_sel_hi:[0,1]\n"
    "v_pk_mul_f16 v99, v65, v46 op_sel_hi:[0,1]\n"
    "v_mul_f32 v84, v84, v83\n"
    "s_waitcnt lgkmcnt(2)\n"
    "v_cmp_gt_u32 vcc, 1, v19\n"
    "s_nop 0\n"
    "s_cbranch_vccnz L31_%=\n"
    "L30_%=:\n"
    "ds_read_b32 v16, v16\n"
    "v_mfma_f32_32x32x16_f16 v[172:187], v[124:127], v[92:95], v[172:187]\n"
    "v_pk_mul_f16 v100, v58, v34 op_sel_hi:[0,1]\n"
    "v_pk_mul_f16 v101, v58, v35 op_sel_hi:[0,1]\n"
    "v_pk_mul_f16 v102, v58, v36 op_sel_hi:[0,1]\n"
    "v_pk_mul_f16 v103, v58, v37 op_sel_hi:[0,1]\n"
    "v_mfma_f32_32x32x16_f16 v[188:203], v[124:127], v[96:99], v[188:203]\n"
    "v_pk_mul_f16 v104, v65, v47 op_sel_hi:[0,1]\n"
    "v_pk_mul_f16 v105, v65, v48 op_sel_hi:[0,1]\n"
    "v_pk_mul_f16 v106, v65, v49 op_sel_hi:[0,1]\n"
    "v_pk_mul_f16 v107, v65, v50 op_sel_hi:[0,1]\n"
    "s_waitcnt lgkmcnt(1)\n"
    "v_mfma_f32_32x32x16_f16 v[172:187], v[128:131], v[100:103], v[172:187]\n"
    "v_pk_mul_f16 v92, v58, v38 op_sel_hi:[0,1]\n"
    "v_mfma_f32_32x32x16_f16 v[188:203], v[128:131], v[104:107], v[188:203]\n"
    "v_pk_mul_f16 v96, v65, v51 op_sel_hi:[0,1]\n"
    "v_mfma_f32_32x32x16_f16 v[172:187], v[132:135], v[92:95], v[172:187]\n"
    "s_nop 0\n"
    "v_mfma_f32_32x32x16_f16 v[188:203], v[132:135], v[96:99], v[188:203]\n"
    "s_nop 9\n"
    "v_mul_f32 v12, v172, v140\n"
    "v_mul_f32 v8, v173, v141\n"
    "v_fmac_f32 v12, v174, v142\n"
    "v_fmac_f32 v8, v175, v143\n"
    "v_fmac_f32 v12, v176, v144\n"
    "v_fmac_f32 v8, v177, v145\n"
    "v_fmac_f32 v12, v178, v146\n"
    "v_fmac_f32 v8, v179, v147\n"
    "v_fmac_f32 v12, v180, v148\n"
    "v_fmac_f32 v8, v181, v149\n"
    "v_fmac_f32 v12, v182, v150\n"
    "v_fmac_f32 v8, v183, v151\n"
    "v_fmac_f32 v12, v184, v152\n"
    "v_add_f32 v12, v12, v8\n"
    "v_mul_f32 v13, v188, v156\n"
    "v_mul_f32 v9, v189, v157\n"
    "v_fmac_f32 v13, v190, v158\n"
    "v_fmac_f32 v9, v191, v159\n"
    "v_fmac_f32 v13, v192, v160\n"
    "v_fmac_f32 v9, v193, v161\n"
    "v_fmac_f32 v13, v194, v162\n"
    "v_fmac_f32 v9, v195, v163\n"
    "v_fmac_f32 v13, v196, v164\n"
    "v_fmac_f32 v9, v197, v165\n"
    "v_fmac_f32 v13, v198, v166\n"
    "v_fmac_f32 v9, v199, v167\n"
    "v_fmac_f32 v13, v200, v168\n"
    "v_add_f32 v13, v13, v9\n"
    "s_nop 1\n"
    "v_permlane32_swap_b32 v12, v13\n"
    "v_add_f32 v14, v12, v13\n"
    "v_fmac_f32 v85, v14, v84\n"
    "s_nop 1\n"
    "v_add_f32_dpp v85, v85, v85 row_ror:8 row_mask:0xf bank_mask:0xf\n"
    "s_nop 1\n"
    "v_add_f32_dpp v85, v85, v85 row_ror:4 row_mask:0xf bank_mask:0xf\n"
    "s_nop 1\n"
    "v_add_f32_dpp v85, v85, v85 row_ror:2 row_mask:0xf bank_mask:0xf\n"
    "s_nop 1\n"
    "v_add_f32_dpp v85, v85, v85 row_ror:1 row_mask:0xf bank_mask:0xf\n"
    "s_waitcnt lgkmcnt(0)\n"
    "v_add_f32 v8, v16, v85\n"
    "v_cvt_pk_bf16_f32 v8, v8, v8\n"
    "v_cmp_class_f32_e64 vcc, v85, s59\n"
    "s_mov_b64 exec, s[62:63]\n"
    "global_store_short v3, v8, s[24:25]\n"
    "s_mov_b64 exec, -1\n"
    "s_cmp_lg_u64 vcc, 0\n"
    "s_cselect_b32 s28, 1, s28\n"
    "v_mov_b32 v13, s28\n"
    "ds_write_b32 v2, v13\n"
    "s_waitcnt lgkmcnt(0)\n"
    "s_barrier\n"
    "ds_read_b32 v15, v15\n"
    "s_waitcnt lgkmcnt(0)\n"
    "v_cmp_ne_u32 vcc, 0, v15\n"
    "s_nop 0\n"
    "s_cmp_lg_u64 vcc, 0\n"
    "s_cselect_b32 s28, 1, 0\n"
    "s_branch LE2_%=\n"
    "L19_%=:\n"
    "ds_read_b32 v19, v17 offset:8\n"
    "s_waitcnt lgkmcnt(0)\n"
    "v_readfirstlane_b32 s55, v19\n"
    "s_nop 0\n"
    "s_cmp_ge_u32 s55, 4\n"
    "s_cbranch_scc1 L20_%=\n"
    "s_sleep 1\n"
    "s_branch L19_%=\n"
    "L20_%=:\n"
    "s_branch L18_%=\n"
    "L22_%=:\n"
    "ds_read_b32 v19, v17 offset:16\n"
    "s_waitcnt lgkmcnt(0)\n"
    "v_readfirstlane_b32 s55, v19\n"
    "s_nop 0\n"
    "s_cmp_ge_u32 s55, 2\n"
    "s_cbranch_scc1 L23_%=\n"
    "s_sleep 1\n"
    "s_branch L22_%=\n"
    "L23_%=:\n"
    "s_branch L21_%=\n"
    "L25_%=:\n"
    "ds_read_b32 v19, v17 offset:24\n"
    "s_waitcnt lgkmcnt(0)\n"
    "v_readfirstlane_b32 s55, v19\n"
    "s_nop 0\n"
    "s_cmp_ge_u32 s55, 2\n"
    "s_cbranch_scc1 L26_%=\n"
    "s_sleep 1\n"
    "s_branch L25_%=\n"
    "L26_%=:\n"
    "s_branch L24_%=\n"
    "L28_%=:\n"
    "ds_read_b32 v19, v17 offset:0\n"
    "s_waitcnt lgkmcnt(0)\n"
    "v_readfirstlane_b32 s55, v19\n"
    "s_nop 0\n"
    "s_cmp_ge_u32 s55, 4\n"
    "s_cbranch_scc1 L29_%=\n"
    "s_sleep 1\n"
    "s_branch L28_%=\n"
    "L29_%=:\n"
    "s_branch L27_%=\n"
    "L31_%=:\n"
    "ds_read_b32 v19, v17 offset:40\n"
    "s_waitcnt lgkmcnt(0)\n"
    "v_readfirstlane_b32 s55, v19\n"
    "s_nop 0\n"
    "s_cmp_ge_u32 s55, 1\n"
    "s_cbranch_scc1 L32_%=\n"
    "s_sleep 1\n"
    "s_branch L31_%=\n"
    "L32_%=:\n"
    "s_branch L30_%=\n"
    "L40_%=:\n"
    "ds_read_b32 v19, v17 offset:32\n"
    "s_waitcnt lgkmcnt(0)\n"
    "v_readfirstlane_b32 s55, v19\n"
    "s_nop 0\n"
    "s_cmp_ge_u32 s55, 4\n"
    "s_cbranch_scc1 L41_%=\n"
    "s_sleep 1\n"
    "s_branch L40_%=\n"
    "L41_%=:\n"
    "s_branch L39_%=\n"
    "LL4_%=:\n"
    "s_barrier\n"
    "v_mov_b32 v17, 153728\n"
    "v_mov_b32 v18, 1\n"
    "s_mov_b32 s28, 0\n"
    "s_waitcnt lgkmcnt(0)\n"
    "s_load_dword s56, s[26:27], 0x0\n"
    "s_waitcnt lgkmcnt(0)\n"
    "s_add_u32 s56, s56, 2654435769\n"
    "LN8_%=:\n"
    "s_setprio 3\n"
    "v_mov_b32 v13, 17\n"
    "s_mov_b64 exec, 1\n"
    "ds_add_u32 v17, v13 offset:12\n"
    "s_mov_b64 exec, -1\n"
    "s_add_u32 s64, s26, 63488\n"
    "s_addc_u32 s65, s27, 0\n"
    "v_min_u32 v10, 25, v1\n"
    "s_and_b32 s44, s2, 1\n"
    "s_lshl_b32 s44, s44, 14\n"
    "s_add_u32 s44, s44, 4096\n"
    "v_lshl_add_u32 v10, v10, 7, s44\n"
    "v_add_u32 v4, 4096, v10\n"
    "s_mul_i32 s52, s31, 9216\n"
    "s_min_u32 s42, s52, 26624\n"
    "s_add_u32 s52, s52, 19456\n"
    "s_add_u32 s42, s42, 113664\n"
    "s_and_b32 s43, s31, 1\n"
    "s_mul_i32 s44, s43, 17408\n"
    "s_mul_i32 s40, s43, 10240\n"
    "s_add_u32 s40, s40, 56320\n"
    "s_add_u32 s44, s44, 77824\n"
    "s_cmp_lt_u32 s31, 2\n"
    "s_cselect_b32 s40, s40, s44\n"
    "s_cselect_b32 s41, 15, 0xff\n"
    "s_cselect_b32 s44, 24, 16\n"
    "s_cselect_b32 s43, 2, 3\n"
    "v_add_u32 v9, 8, v17\n"
    "v_add_u32 v12, s44, v17\n"
    "v_lshl_add_u32 v5, s43, 12, v10\n"
    "s_cselect_b32 s43, 11, 18\n"
    "s_mov_b32 s54, 9\n"
    "s_mov_b32 s53, 0\n"
    "s_mov_b32 s45, 65536\n"
    "s_mov_b32 s59, 0\n"
    "s_cmp_lt_u32 s31, 2\n"
    "s_cselect_b32 s59, 1, 0\n"
    "s_cselect_b32 s44, 28, 8\n"
    "v_add_u32 v9, s44, v17\n"
    "s_cbranch_scc1 L49_%=\n"
    "s_mul_i32 s52, s31, 18432\n"
    "s_add_u32 s52, s52, 4294949888\n"
    "s_mov_b32 s54, 18\n"
    "s_mov_b64 exec, 1\n"
    "ds_add_u32 v17, v18 offset:8\n"
    "s_mov_b64 exec, -1\n"
    "global_load_dwordx2 v[6:7], v4, s[26:27] sc1\n"
    "L49_%=:\n"
    "LJ9_%=:\n"
    "L50_%=:\n"
    "s_call_b64 s[50:51], LT33_%=\n"
    "s_cmp_eq_u32 s59, 6\n"
    "s_cbranch_scc1 L51_%=\n"
    "s_sleep 4\n"
    "s_branch L50_%=\n"
    "L51_%=:\n"
    "s_setprio 3\n"
    "s_cmp_lg_u32 s31, 2\n"
    "s_cbranch_scc1 L58_%=\n"
    "s_lshl_b32 s44, s2, 2\n"
    "v_mov_b32 v4, s44\n"
    "v_mov_b32 v5, s56\n"
    "s_mov_b64 exec, 1\n"
    "global_store_dword v4, v5, s[26:27] offset:1024 sc1\n"
    "s_mov_b64 exec, -1\n"
    "L58_%=:\n"
    "s_cmp_lg_u32 s31, 0\n"
    "s_cbranch_scc1 L59_%=\n"
    "v_mov_b32 v10, 0\n"
    "global_load_ushort v20, v10, s[22:23]\n"
    "s_lshl_b32 s44, s2, 10\n"
    "s_add_u32 s44, s44, 262144\n"
    "v_lshlrev_b32 v4, 3, v1\n"
    "v_add_u32 v4, s44, v4\n"
    "v_min_u32 v5, 47, v1\n"
    "v_lshlrev_b32 v5, 3, v5\n"
    "v_add_u32 v5, s44, v5\n"
    "s_mov_b32 s45, 65536\n"
    "L60_%=:\n"
    "global_load_dwordx2 v[6:7], v4, s[26:27] sc1\n"
    "global_load_dwordx2 v[8:9], v5, s[26:27] offset:512 sc1\n"
    "s_waitcnt vmcnt(0)\n"
    "v_cmp_ne_u32 vcc, s56, v7\n"
    "v_cmp_ne_u32 s[38:39], s56, v9\n"
    "s_nop 0\n"
    "s_or_b64 s[38:39], s[38:39], vcc\n"
    "s_cbranch_scc0 L61_%=\n"
    "s_sleep 3\n"
    "s_sub_u32 s45, s45, 1\n"
    "s_cmp_lg_u32 s45, 0\n"
    "s_cbranch_scc1 L60_%=\n"
    "s_mov_b32 s28, 1\n"
    "L61_%=:\n"
    "s_mov_b32 s38, -1\n"
    "s_mov_b32 s39, 0xffff\n"
    "v_cndmask_b32 v8, 0, v8, s[38:39]\n"
    "v_add_f32 v21, v6, v8\n"
    "v_mov_b32 v9, v21\n"
    "s_nop 1\n"
    "v_permlane32_swap_b32 v21, v9\n"
    "v_add_f32 v21, v21, v9\n"
    "v_mov_b32 v9, v21\n"
    "s_nop 1\n"
    "v_permlane16_swap_b32 v21, v9\n"
    "v_add_f32 v21, v21, v9\n"
    "v_lshlrev_b32 v20, 16, v20\n"
    "v_add_f32 v21, v21, v20\n"
    "v_and_b32 v14, 15, v1\n"
    "v_lshlrev_b32 v14, 2, v14\n"
    "v_add_u32 v14, 153600, v14\n"
    "ds_write_b32 v14, v21\n"
    "s_mov_b64 exec, 1\n"
    "ds_add_u32 v17, v18 offset:40\n"
    "s_mov_b64 exec, -1\n"
    "L59_%=:\n"
    "s_lshl_b32 s36, s2, 2\n"
    "s_add_u32 s36, s36, s31\n"
    "s_cmp_lg_u32 s36, 1021\n"
    "s_cbranch_scc1 L62_%=\n"
    "s_mov_b32 s45, 65536\n"
    "L63_%=:\n"
    "global_load_dwordx4 v[6:9], v2, s[26:27] offset:1024 sc1\n"
    "s_waitcnt vmcnt(0)\n"
    "v_xor_b32 v6, s56, v6\n"
    "v_xor_b32 v7, s56, v7\n"
    "v_xor_b32 v8, s56, v8\n"
    "v_xor_b32 v9, s56, v9\n"
    "v_or3_b32 v6, v6, v7, v8\n"
    "v_or_b32 v6, v6, v9\n"
    "v_cmp_ne_u32 vcc, 0, v6\n"
    "s_nop 0\n"
    "s_cmp_eq_u64 vcc, 0\n"
    "s_cbranch_scc1 L64_%=\n"
    "s_sleep 8\n"
    "s_sub_u32 s45, s45, 1\n"
    "s_cmp_lg_u32 s45, 0\n"
    "s_cbranch_scc1 L63_%=\n"
    "L64_%=:\n"
    "v_mov_b32 v4, 0\n"
    "v_mov_b32 v5, s56\n"
    "s_mov_b64 exec, 1\n"
    "global_store_dword v4, v5, s[26:27] sc1\n"
    "s_mov_b64 exec, -1\n"
    "L62_%=:\n"
    "s_lshl_b32 s41, s30, 2\n"
    "s_add_u32 s41, s41, 153664\n"
    "v_mov_b32 v11, s41\n"
    "v_and_b32 v6, 7, v1\n"
    "v_lshlrev_b32 v6, 2, v6\n"
    "v_add_u32 v6, 153664, v6\n"
    "v_mov_b32 v13, s28\n"
    "ds_write_b32 v11, v13\n"
    "s_waitcnt lgkmcnt(0)\n"
    "s_barrier\n"
    "ds_read_b32 v6, v6\n"
    "s_waitcnt lgkmcnt(0)\n"
    "v_cmp_ne_u32 vcc, 0, v6\n"
    "s_nop 0\n"
    "s_cmp_lg_u64 vcc, 0\n"
    "s_cselect_b32 s28, 1, 0\n"
    "LE2_%=:\n"
    "s_waitcnt lgkmcnt(0)\n"
    "s_setprio 0\n"
    "s_cmp_eq_u32 s28, 0\n"
    "s_cbranch_scc1 L65_%=\n"
    "s_waitcnt vmcnt(0)\n"
    "L65_%=:\n"
    : "={s28}"(bad), "={s[8:9]}"(idx), "={s[10:11]}"(table), "={s[12:13]}"(W), "={s[14:15]}"(wout), "={s[16:17]}"(w1), "={s[18:19]}"(b1),
      "={s[20:21]}"(w2), "={s[22:23]}"(bout), "={s[24:25]}"(out)
    : "{v0}"(tid), "{s[0:1]}"(kp), "{s2}"(blk), "s"((unsigned)(size_t)smem)
    : "v1", "v2", "v3", "v4", "v5", "v6", "v7", "v8", "v9", "v10", "v11", "v12", "v13", "v14", "v15", "v16", "v17", "v18", "v19", "v20", "v21", "v22", "v23", "v24", "v25", "v26", "v27", "v28", "v29", "v30", "v31", "v32", "v33", "v34", "v35", "v36", "v37", "v38", "v39", "v40", "v41", "v42", "v43", "v44", "v45", "v46", "v47", "v48", "v49", "v50", "v51", "v52", "v53", "v54", "v55", "v56", "v57", "v58", "v59", "v60", "v61", "v62", "v63", "v64", "v65", "v66", "v67", "v68", "v69", "v70", "v71", "v72", "v73", "v74", "v75", "v76", "v77", "v78", "v79", "v80", "v81", "v82", "v83", "v84", "v85", "v86", "v87", "v88", "v89", "v90", "v91", "v92", "v93", "v94", "v95", "v96", "v97", "v98", "v99", "v100", "v101", "v102", "v103", "v104", "v105", "v106", "v107", "v108", "v109", "v110", "v111", "v112", "v113", "v114", "v115", "v116", "v117", "v118", "v119", "v120", "v121", "v122", "v123", "v124", "v125", "v126", "v127", "v128", "v129", "v130", "v131", "v132", "v133", "v134", "v135", "v136", "v137", "v138", "v139", "v140", "v141", "v142", "v143", "v144", "v145", "v146", "v147", "v148", "v149", "v150", "v151", "v152", "v153", "v154", "v155", "v156", "v157", "v158", "v159", "v160", "v161", "v162", "v163", "v164", "v165", "v166", "v167", "v168", "v169", "v170", "v171", "v172", "v173", "v174", "v175", "v176", "v177", "v178", "v179", "v180", "v181", "v182", "v183", "v184", "v185", "v186", "v187", "v188", "v189", "v190", "v191", "v192", "v193", "v194", "v195", "v196", "v197", "v198", "v199", "v200", "v201", "v202", "v203", "v204", "v205", "v206", "v207", "v208", "v209", "v210", "v211", "v212", "v213", "v214", "v215", "v216", "v217", "v218", "v219", "v220", "v221", "v222", "v223", "v224", "v225", "v226", "v227", "v228", "v229", "v230", "v231", "v232", "v233", "v234", "v235", "v236", "v237", "v238", "v239", "v240", "v241", "v242", "v243", "v244", "v245", "v246", "v247", "v248", "v249", "v250", "v251", "s26", "s27", "s29", "s30", "s31", "s36", "s37", "s38", "s39", "s40", "s41", "s42", "s43", "s44", "s45", "s46", "s47", "s48", "s49", "s50", "s51", "s52", "s53", "s54", "s55", "s56", "s57", "s58", "s59", "s60", "s61", "s62", "s63", "s64", "s65", "vcc", "scc", "memory");
  if (bad) {
    float* x0 = (float*)smem;
    float* xk = x0 + 26 * 256;
    float* xn = xk + 26 * 256;
    float* red = xn + 26 * 256;
    const int col = tid & 255, hf = tid >> 8;
    __syncthreads();
    for (int f = hf; f < 26; f += 2) {
      float v = bf2f(table[idx[(blk * 16 + (col >> 4)) * 26 + f] * 16 + (col & 15)]);
      x0[f * 256 + col] = v; xk[f * 256 + col] = v;
    }
    __syncthreads();
    float p = 0.f;
    for (int n = (col & 15) + 16 * hf; n < 416; n += 32) {
      float s = bf2f(b1[n]);
      for (int k = 0; k < 416; ++k) s = fmaf(x0[(k >> 4) * 256 + (col & 240) + (k & 15)], bf2f(w1[k * 416 + n]), s);
      const float g = 0.5f * s * (1.f + tanhf(0.7978845608028654f * (s + 0.044715f * s * s * s)));
      p = fmaf(g, bf2f(w2[n]), p);
    }
    for (int l = 0; l < 6; ++l) {
      for (int h = hf * 13; h < hf * 13 + 13; ++h) {
        float s = 0.f;
        for (int i = 0; i < 26; ++i) {
          float m = 0.f;
          for (int j = 0; j < 26; ++j) m = fmaf(bf2f(W[((l * 26 + h) * 26 + i) * 26 + j]), x0[j * 256 + col], m);
          s = fmaf(m, xk[i * 256 + col], s);
        }
        xn[h * 256 + col] = s;
        p = fmaf(s, bf2f(wout[l * 26 + h]), p);
      }
      __syncthreads();
      float* tsw = xk; xk = xn; xn = tsw;
    }
    red[tid] = p;
    __syncthreads();
    if (tid < 16) {
      float s = bf2f(bout[0]);
      for (int k = 0; k < 16; ++k) s += red[tid * 16 + k] + red[256 + tid * 16 + k];
      out[blk * 16 + tid] = __float2bfloat16(s);
    }
  }
}

extern "C" void kernel_launch(void* const* d_in, const int* in_sizes, int n_in,
                              void* d_out, int out_size, void* d_ws, size_t ws_size,
                              hipStream_t stream) {
    xdfm_kernel<<<256, 512, 0, stream>>>((const int*)d_in[0], (const unsigned short*)d_in[1], (const unsigned short*)d_in[2],
      (const unsigned short*)d_in[3], (const unsigned short*)d_in[4], (const unsigned short*)d_in[5], (const unsigned short*)d_in[6],
      (const unsigned short*)d_in[7], (__hip_bfloat16*)d_out, (char*)d_ws);
}
```
